```python
import torch

def _softmax(x, axis=-1):
    return torch.softmax(x, dim=axis)

def _gelu(x):
    return torch.nn.functional.gelu(x, approximate='tanh')

def _silu(x):
    return torch.nn.functional.silu(x)

def _sigmoid(x):
    return torch.sigmoid(x)

def _layernorm(x, axis=-1, eps=1e-05):
    m = x.mean(dim=axis, keepdim=True)
    v = x.var(dim=axis, keepdim=True, unbiased=False)
    return (x - m) / torch.sqrt(v + eps)

def _scatter_add(tgt, idx, src, axis=0):
    return tgt.clone().index_add_(axis, idx, src)

def _segment_sum(src, seg, n):
    out = torch.zeros((n,) + src.shape[1:], dtype=src.dtype)
    return out.index_add_(0, seg, src)

def _pad(x, pw):
    flat = tuple((v for pair in reversed(pw) for v in pair))
    return torch.nn.functional.pad(x, flat)
kernel_name = 'adaLN_cond__ssm_conv_h3__mixer_gmlp__prompt'

def setup_inputs():

    _w = lambda a: (lambda t: t.to(torch.bfloat16) if t.is_floating_point() else t)(torch.as_tensor(a))

    def _A_setup():
        B, S, D = (1, 2048, 448)
        ks = 4
        return {'q': _w((lambda *s: torch.randn(*s, dtype=torch.bfloat16) * 0.05)(B, S, D)), 'k': _w((lambda *s: torch.randn(*s, dtype=torch.bfloat16) * 1.6)(B, S, D)), 'v': _w((lambda *s: torch.randn(*s, dtype=torch.bfloat16) * 1.6)(B, S, D)), 'shift_k': _w((lambda *s: torch.randn(*s, dtype=torch.bfloat16) * 1.6)(D, ks) * 0.5), 'ssm_k': _w((lambda *s: torch.randn(*s, dtype=torch.bfloat16) * 1.6)(D, S) * (1.0 / S ** 0.5)), 'ssm_D': _w((lambda *s: torch.randn(*s, dtype=torch.bfloat16) * 1.6)(D))}
    _A_in = _A_setup()

    def _B_setup():
        B, S, D, F = (1, 2048, 448, 1792)
        Fh = F // 2
        return {'x': _w((lambda *s: torch.randn(*s, dtype=torch.bfloat16) * 0.05)(B, S, D)), 'w_in': _w((lambda *s: torch.randn(*s, dtype=torch.bfloat16) * 0.8)(D, 2 * Fh)), 'w_sp': _w(0.01 * (lambda *s: torch.randn(*s, dtype=torch.bfloat16) * 0.8)(S, S)), 'b_sp': _w(1.0 + 0.01 * (lambda *s: torch.randn(*s, dtype=torch.bfloat16) * 0.1)(S)), 'w_out': _w((lambda *s: torch.randn(*s, dtype=torch.bfloat16) * 0.1)(Fh, D))}
    _B_in = _B_setup()
    return {'A_q': _A_in['q'], 'A_k': _A_in['k'], 'A_v': _A_in['v'], 'A_shift_k': _A_in['shift_k'], 'A_ssm_k': _A_in['ssm_k'], 'A_ssm_D': _A_in['ssm_D'], 'B_x': _B_in['x'], 'B_w_in': _B_in['w_in'], 'B_w_sp': _B_in['w_sp'], 'B_b_sp': _B_in['b_sp'], 'B_w_out': _B_in['w_out'], 'c': _w((lambda *s: torch.randn(*s, dtype=torch.bfloat16) * 0.1)(1, 448) * 8.0), 'w_mod': _w((lambda *s: torch.randn(*s, dtype=torch.bfloat16) * 0.1)(448, 6 * 448) * 448 ** (-0.5) * 8.0)}

def reference(A_q, A_k, A_v, A_shift_k, A_ssm_k, A_ssm_D, B_x, B_w_in, B_w_sp, B_b_sp, B_w_out, c, w_mod):

    def _A_ref(q, k, v, shift_k, ssm_k, ssm_D):
        B, S, D = q.shape
        ks = shift_k.shape[-1]
        k_pad = _pad(k, ((0, 0), (ks - 1, 0), (0, 0)))
        k_shift = torch.zeros_like(k)
        for t in range(ks):
            k_shift = k_shift + k_pad[:, t:t + S, :] * shift_k[:, ks - 1 - t][None, None, :]
        kv = k_shift * v
        L = 2 * S
        kv_pad = _pad(kv, ((0, 0), (0, S), (0, 0)))
        kf_pad = _pad(ssm_k, ((0, 0), (0, S)))
        kv_f = (lambda x, axes=(-2, -1): torch.fft.rfft2(x.float(), dim=axes))(kv_pad[:, None, :, :], axes=(1, 2))
        kf_f = (lambda x, axes=(-2, -1): torch.fft.rfft2(x.float(), dim=axes))(kf_pad[:, None, :], axes=(1, 2))
        kf_f = (lambda x, *d: x.permute(*d))(kf_f, 1, 2, 0)
        y_f = kv_f * kf_f[None, :, :, :]
        y = (lambda x, s, axes=(-2, -1): torch.fft.irfft2(x, s=s, dim=axes).to(torch.bfloat16))(y_f, (1, L), axes=(1, 2))[:, 0, :S, :]
        y = y + kv * ssm_D[None, None, :]
        return q * y

    def _B_ref(x, w_in, w_sp, b_sp, w_out):
        B, S, D = x.shape
        F = w_in.shape[-1]
        Fh = F // 2
        z = _gelu(torch.einsum('bsd,df->bsf', x, w_in))
        z1 = z[..., :Fh].clone()
        z2 = _layernorm(z[..., Fh:], axis=-1)
        g = torch.einsum('ts,bsf->btf', w_sp, z2) + b_sp[None, :, None]
        return torch.einsum('bsf,fd->bsd', z1 * g, w_out)
    x = A_q
    mod = (_silu(c) @ w_mod).reshape(c.shape[0], 6, 1, -1)
    sa, ca, ga, sb, cb, gb = (mod[:, 0].clone(), mod[:, 1].clone(), mod[:, 2].clone(), mod[:, 3].clone(), mod[:, 4].clone(), mod[:, 5].clone())
    h = _layernorm(x, axis=-1) * (1 + ca) + sa
    x = x + ga * _A_ref(h, A_k, A_v, A_shift_k, A_ssm_k, A_ssm_D)
    h = _layernorm(x, axis=-1) * (1 + cb) + sb
    x = x + gb * _B_ref(h, B_w_in, B_w_sp, B_b_sp, B_w_out)
    return x
```

```cpp
#include <hip/hip_runtime.h>
#include <hip/hip_bf16.h>
#include <stdint.h>

typedef unsigned short u16;
typedef short s16x8 __attribute__((ext_vector_type(8)));
typedef short s16x4 __attribute__((ext_vector_type(4)));
typedef float f32x4 __attribute__((ext_vector_type(4)));
typedef unsigned int u32x4 __attribute__((ext_vector_type(4)));
typedef __attribute__((address_space(3))) s16x4* lds_v4p;

constexpr int S = 2048, D = 448, F = 1792, FH = 896;

__device__ __forceinline__ float bf2f(u16 h) { return __uint_as_float(((unsigned)h) << 16); }
__device__ __forceinline__ u16 f2bf(float f) {
  unsigned u = __float_as_uint(f);
  u += 0x7fffu + ((u >> 16) & 1u);
  return (u16)(u >> 16);
}
__device__ __forceinline__ float rb(float f) { return bf2f(f2bf(f)); }

__device__ __forceinline__ s16x8 tr_frag(const u16* base_k0, int ldk  , int lane) {
  const int g = lane >> 4, q = (lane & 15) >> 2, p = lane & 3;
  const u16* p0 = base_k0 + (8 * g + q) * ldk + 4 * p;
  s16x4 lo = __builtin_amdgcn_ds_read_tr16_b64_v4i16((lds_v4p)(p0));
  s16x4 hi = __builtin_amdgcn_ds_read_tr16_b64_v4i16((lds_v4p)(p0 + 4 * ldk));
  s16x8 r;
  r[0] = lo[0]; r[1] = lo[1]; r[2] = lo[2]; r[3] = lo[3];
  r[4] = hi[0]; r[5] = hi[1]; r[6] = hi[2]; r[7] = hi[3];
  return r;
}

__global__ __launch_bounds__(256) void k1_prep(const u16* __restrict__ q, const u16* __restrict__ k,
                                               const u16* __restrict__ v, const u16* __restrict__ shk,
                                               const u16* __restrict__ cvec, const u16* __restrict__ wmod,
                                               u16* __restrict__ kvT, u16* __restrict__ xT,
                                               float* __restrict__ xstat, float* __restrict__ modf,
                                               float* __restrict__ zsum) {
  __shared__ __attribute__((aligned(16))) char smem[2 * 64 * 72 * 2];
  const int b = blockIdx.x, tid = threadIdx.x;
  if (b < 224) {
    u16* Tkv = (u16*)smem;
    u16* Tx = Tkv + 64 * 72;
    const int st = b / 7, dt = b % 7;
    const int s0 = st * 64, d0 = dt * 64;
    const int c = tid & 7, r = tid >> 3;
    u16 sh[32];
    {
      const u32x4* sp = (const u32x4*)(shk + (size_t)(d0 + 8 * c) * 4);
#pragma unroll
      for (int i = 0; i < 4; i++) *(u32x4*)&sh[8 * i] = sp[i];
    }
#pragma unroll
    for (int pass = 0; pass < 2; pass++) {
      const int row = r + 32 * pass;
      const int s = s0 + row;
      u16 qq[8], vv[8], kk[4][8];
      *(u32x4*)qq = *(const u32x4*)(q + (size_t)s * D + d0 + 8 * c);
      *(u32x4*)vv = *(const u32x4*)(v + (size_t)s * D + d0 + 8 * c);
#pragma unroll
      for (int j = 0; j < 4; j++) {
        if (s - j >= 0) *(u32x4*)kk[j] = *(const u32x4*)(k + (size_t)(s - j) * D + d0 + 8 * c);
        else { u32x4 z = {0, 0, 0, 0}; *(u32x4*)kk[j] = z; }
      }
#pragma unroll
      for (int e = 0; e < 8; e++) {
        float acc = 0.f;
#pragma unroll
        for (int j = 3; j >= 0; j--) acc = rb(acc + rb(bf2f(kk[j][e]) * bf2f(sh[e * 4 + j])));
        Tkv[(8 * c + e) * 72 + row] = f2bf(acc * bf2f(vv[e]));
        Tx[(8 * c + e) * 72 + row] = qq[e];
      }
    }
    __syncthreads();
#pragma unroll
    for (int pass = 0; pass < 2; pass++) {
      const int dr = r + 32 * pass;
      *(u32x4*)(kvT + (size_t)(d0 + dr) * S + s0 + 8 * c) = *(const u32x4*)(Tkv + dr * 72 + 8 * c);
      *(u32x4*)(xT + (size_t)(d0 + dr) * S + s0 + 8 * c) = *(const u32x4*)(Tx + dr * 72 + 8 * c);
    }
  } else if (b < 288) {
    const int wave = tid >> 6, lane = tid & 63;
    const int rb0 = (b - 224) * 32 + wave * 8;
    for (int rr = 0; rr < 8; rr++) {
      const int s = rb0 + rr;
      float xv[8];
      float sum = 0.f;
      if (lane < 56) {
        u16 t[8];
        *(u32x4*)t = *(const u32x4*)(q + (size_t)s * D + lane * 8);
#pragma unroll
        for (int e = 0; e < 8; e++) { xv[e] = bf2f(t[e]); sum += xv[e]; }
      } else {
#pragma unroll
        for (int e = 0; e < 8; e++) xv[e] = 0.f;
      }
#pragma unroll
      for (int o = 32; o >= 1; o >>= 1) sum += __shfl_xor(sum, o);
      const float mean = sum * (1.f / D);
      float sq = 0.f;
      if (lane < 56) {
#pragma unroll
        for (int e = 0; e < 8; e++) { float dd = xv[e] - mean; sq += dd * dd; }
      }
#pragma unroll
      for (int o = 32; o >= 1; o >>= 1) sq += __shfl_xor(sq, o);
      if (lane == 0) {
        const float var = rb(sq * (1.f / D));
        xstat[2 * s] = rb(mean);
        xstat[2 * s + 1] = rb(sqrtf(rb(var + 1e-5f)));
      }
      if (rr < 2) {
        const int idx = ((b - 224) * 256 + tid) * 2 + rr;
        if (idx < 2 * S) zsum[idx] = 0.f;
      }
    }
  } else {
    float* red = (float*)smem;
    const int n0 = (b - 288) * 64;
    const int c8 = tid & 7, ks = tid >> 3;
    float acc[8];
#pragma unroll
    for (int e = 0; e < 8; e++) acc[e] = 0.f;
    for (int kk = 0; kk < 14; kk++) {
      const int kr = ks * 14 + kk;
      const float cv = bf2f(cvec[kr]);
      const float sc = rb(cv / (1.f + __expf(-cv)));
      u16 t[8];
      *(u32x4*)t = *(const u32x4*)(wmod + (size_t)kr * (6 * D) + n0 + 8 * c8);
#pragma unroll
      for (int e = 0; e < 8; e++) acc[e] += sc * bf2f(t[e]);
    }
#pragma unroll
    for (int e = 0; e < 8; e++) red[ks * 64 + c8 * 8 + e] = acc[e];
    __syncthreads();
    if (tid < 64) {
      float sum = 0.f;
      for (int i = 0; i < 32; i++) sum += red[i * 64 + tid];
      modf[n0 + tid] = rb(sum);
    }
  }
}

__global__ __launch_bounds__(256) void k2_conv(const u16* __restrict__ kvT, const u16* __restrict__ xT,
                                               const u16* __restrict__ ssmk, const u16* __restrict__ ssmD,
                                               const float* __restrict__ xstat, const float* __restrict__ modf,
                                               u16* __restrict__ x1T) {
  __shared__ __attribute__((aligned(16))) u16 KFs[2208];
  __shared__ __attribute__((aligned(16))) u16 Rs[4112];
  __shared__ __attribute__((aligned(16))) float ybuf[4][2112];
  const int d = blockIdx.x, tid = threadIdx.x;
  const int wave = tid >> 6, lane = tid & 63;
  {
    u32x4 z = {0, 0, 0, 0};
    if (tid < 16) *(u32x4*)&KFs[8 * tid] = z;
    if (tid < 4) *(u32x4*)&KFs[2176 + 8 * tid] = z;
    *(u32x4*)&KFs[128 + 8 * tid] = *(const u32x4*)(ssmk + (size_t)d * S + 8 * tid);
    u16 t[8], tr[8];
    *(u32x4*)t = *(const u32x4*)(kvT + (size_t)d * S + 8 * tid);
#pragma unroll
    for (int e = 0; e < 8; e++) tr[7 - e] = t[e];
    *(u32x4*)&Rs[2040 - 8 * tid + 8] = *(u32x4*)tr;
    if (tid == 0) *(u32x4*)&Rs[0] = z;
    for (int i = tid; i < 257; i += 256) *(u32x4*)&Rs[2056 + 8 * i] = z;
  }
  __syncthreads();
  const int m = lane & 15, h = lane >> 4;
  f32x4 acc[8];
#pragma unroll
  for (int p = 0; p < 8; p++) acc[p] = (f32x4){0.f, 0.f, 0.f, 0.f};
  const int tbeg = wave == 0 ? 0 : 17 + 16 * (wave - 1);
  const int tend = wave == 0 ? 17 : tbeg + 16;
  for (int t = tbeg; t < tend; t++) {
    const int q0 = -128 + 32 * t;
    const s16x8 a = *(const s16x8*)&KFs[8 * m + q0 + 8 * h + 128];
    const int basep = 2040 - 128 * m + q0 + 8 * h;
    const u32x4 w0 = *(const u32x4*)&Rs[basep + 8];
    const u32x4 w1 = *(const u32x4*)&Rs[basep + 16];
    unsigned W[8] = {w0[0], w0[1], w0[2], w0[3], w1[0], w1[1], w1[2], w1[3]};
#pragma unroll
    for (int p = 0; p < 8; p++) {
      const int sft = 7 - p;
      u32x4 fr;
      if ((sft & 1) == 0) {
#pragma unroll
        for (int i = 0; i < 4; i++) fr[i] = W[sft / 2 + i];
      } else {
#pragma unroll
        for (int i = 0; i < 4; i++) fr[i] = __builtin_amdgcn_alignbit(W[(sft - 1) / 2 + i + 1], W[(sft - 1) / 2 + i], 16);
      }
      acc[p] = __builtin_amdgcn_mfma_f32_16x16x32_bf16(a, *(s16x8*)&fr, acc[p], 0, 0, 0);
    }
  }
  {
    const int i = lane & 15;
#pragma unroll
    for (int reg = 0; reg < 4; reg++) {
      const int mm = 4 * h + reg;
      const int s = 128 * i + 8 * mm;
      f32x4 v0 = {acc[0][reg], acc[1][reg], acc[2][reg], acc[3][reg]};
      f32x4 v1 = {acc[4][reg], acc[5][reg], acc[6][reg], acc[7][reg]};
      *(f32x4*)&ybuf[wave][s + 4 * i] = v0;
      *(f32x4*)&ybuf[wave][s + 4 * i + 4] = v1;
    }
  }
  __syncthreads();
  {
    const int s0 = 8 * tid;
    const int idx = s0 + 4 * (s0 >> 7);
    float y[8];
#pragma unroll
    for (int e = 0; e < 8; e++) y[e] = 0.f;
#pragma unroll
    for (int w = 0; w < 4; w++) {
      f32x4 v0 = *(const f32x4*)&ybuf[w][idx];
      f32x4 v1 = *(const f32x4*)&ybuf[w][idx + 4];
#pragma unroll
      for (int e = 0; e < 4; e++) { y[e] += v0[e]; y[4 + e] += v1[e]; }
    }
    u16 kvv[8], xv[8], o[8];
    *(u32x4*)kvv = *(const u32x4*)(kvT + (size_t)d * S + s0);
    *(u32x4*)xv = *(const u32x4*)(xT + (size_t)d * S + s0);
    const float Dd = bf2f(ssmD[d]);
    const float sa = modf[d], opca = rb(1.f + modf[D + d]), ga = modf[2 * D + d];
#pragma unroll
    for (int e = 0; e < 8; e++) {
      const float mean = xstat[2 * (s0 + e)], sd = xstat[2 * (s0 + e) + 1];
      const float yy = rb(y[e]);
      const float y2 = rb(yy + rb(bf2f(kvv[e]) * Dd));
      const float x = bf2f(xv[e]);
      const float ln = rb(rb(x - mean) / sd);
      const float hh = rb(rb(ln * opca) + sa);
      const float a = rb(hh * y2);
      o[e] = f2bf(x + rb(ga * a));
    }
    *(u32x4*)(x1T + (size_t)d * S + s0) = *(u32x4*)o;
  }
}

__device__ __forceinline__ float gelu_tanh(float x) {
  const float y = 0.7978845608028654f * (x + 0.044715f * x * x * x);
  const float t = 1.f - 2.f / (__expf(2.f * y) + 1.f);
  return 0.5f * x * (1.f + t);
}

__global__ __launch_bounds__(256) void k3_gemm1(const u16* __restrict__ x1T, const u16* __restrict__ win,
                                                const float* __restrict__ modf, u16* __restrict__ z,
                                                float* __restrict__ zsum) {
  __shared__ __attribute__((aligned(16))) u16 As[448 * 72];
  __shared__ __attribute__((aligned(16))) u16 Bs[64 * 136];
  __shared__ float red[256];
  __shared__ float smean[64], ssd[64];
  const int tid = threadIdx.x, wave = tid >> 6, lane = tid & 63;
  const int m0 = (blockIdx.x & 31) * 64, n0 = (blockIdx.x >> 5) * 128;
  {
    const int c = tid & 7, r = tid >> 3;
#pragma unroll
    for (int pass = 0; pass < 14; pass++) {
      const int kr = r + 32 * pass;
      *(u32x4*)&As[kr * 72 + 8 * c] = *(const u32x4*)(x1T + (size_t)kr * S + m0 + 8 * c);
    }
  }
  __syncthreads();
  {
    const int mm = tid & 63, kq = tid >> 6;
    float sum = 0.f;
    for (int kk = 0; kk < 112; kk++) sum += bf2f(As[(kq * 112 + kk) * 72 + mm]);
    red[tid] = sum;
    __syncthreads();
    const float mean = (red[mm] + red[64 + mm] + red[128 + mm] + red[192 + mm]) * (1.f / D);
    __syncthreads();
    float sq = 0.f;
    for (int kk = 0; kk < 112; kk++) { float dd = bf2f(As[(kq * 112 + kk) * 72 + mm]) - mean; sq += dd * dd; }
    red[tid] = sq;
    __syncthreads();
    if (tid < 64) {
      const float var = rb((red[mm] + red[64 + mm] + red[128 + mm] + red[192 + mm]) * (1.f / D));
      smean[mm] = rb(mean);
      ssd[mm] = rb(sqrtf(rb(var + 1e-5f)));
    }
    __syncthreads();
    const float mu = smean[mm], sd = ssd[mm];
    for (int kk = 0; kk < 112; kk++) {
      const int kr = kq * 112 + kk;
      const float x = bf2f(As[kr * 72 + mm]);
      const float ln = rb(rb(x - mu) / sd);
      const float hh = rb(rb(ln * rb(1.f + modf[4 * D + kr])) + modf[3 * D + kr]);
      As[kr * 72 + mm] = f2bf(hh);
    }
  }
  const int wr = wave >> 1, wc = wave & 1;
  f32x4 acc[2][4];
#pragma unroll
  for (int i = 0; i < 2; i++)
#pragma unroll
    for (int j = 0; j < 4; j++) acc[i][j] = (f32x4){0.f, 0.f, 0.f, 0.f};
  const int bc = tid & 15, br = tid >> 4;
  u32x4 breg[4];
#pragma unroll
  for (int p = 0; p < 4; p++) breg[p] = *(const u32x4*)(win + (size_t)(br + 16 * p) * F + n0 + 8 * bc);
  for (int kt = 0; kt < 7; kt++) {
    __syncthreads();
#pragma unroll
    for (int p = 0; p < 4; p++) *(u32x4*)&Bs[(br + 16 * p) * 136 + 8 * bc] = breg[p];
    __syncthreads();
    if (kt + 1 < 7) {
#pragma unroll
      for (int p = 0; p < 4; p++) breg[p] = *(const u32x4*)(win + (size_t)((kt + 1) * 64 + br + 16 * p) * F + n0 + 8 * bc);
    }
#pragma unroll
    for (int ks = 0; ks < 2; ks++) {
      s16x8 af[2], bfr[4];
#pragma unroll
      for (int i = 0; i < 2; i++) af[i] = tr_frag(&As[(kt * 64 + ks * 32) * 72 + wr * 32 + i * 16], 72, lane);
#pragma unroll
      for (int j = 0; j < 4; j++) bfr[j] = tr_frag(&Bs[(ks * 32) * 136 + wc * 64 + j * 16], 136, lane);
#pragma unroll
      for (int i = 0; i < 2; i++)
#pragma unroll
        for (int j = 0; j < 4; j++) acc[i][j] = __builtin_amdgcn_mfma_f32_16x16x32_bf16(af[i], bfr[j], acc[i][j], 0, 0, 0);
    }
  }
  const int fr = lane & 15, fq = lane >> 4;
#pragma unroll
  for (int i = 0; i < 2; i++) {
#pragma unroll
    for (int reg = 0; reg < 4; reg++) {
      const int row = m0 + wr * 32 + i * 16 + fq * 4 + reg;
      float s1 = 0.f, s2 = 0.f;
#pragma unroll
      for (int j = 0; j < 4; j++) {
        const int col = n0 + wc * 64 + j * 16 + fr;
        const float g = rb(gelu_tanh(rb(acc[i][j][reg])));
        z[(size_t)row * F + col] = f2bf(g);
        s1 += g; s2 += g * g;
      }
      if (n0 >= FH) {
#pragma unroll
        for (int o = 1; o < 16; o <<= 1) { s1 += __shfl_xor(s1, o); s2 += __shfl_xor(s2, o); }
        if (fr == 0) { atomicAdd(&zsum[2 * row], s1); atomicAdd(&zsum[2 * row + 1], s2); }
      }
    }
  }
}

__global__ __launch_bounds__(256) void k4_gemm2(const u16* __restrict__ wsp, const u16* __restrict__ z,
                                                const float* __restrict__ zsum, const u16* __restrict__ bsp,
                                                u16* __restrict__ u) {
  __shared__ __attribute__((aligned(16))) u16 As[128 * 72];
  __shared__ __attribute__((aligned(16))) u16 Bs[64 * 72];
  const int tid = threadIdx.x, wave = tid >> 6, lane = tid & 63;
  const int m0 = (blockIdx.x & 15) * 128, n0 = (blockIdx.x >> 4) * 64;
  const int wr = wave >> 1, wc = wave & 1;
  f32x4 acc[4][2];
#pragma unroll
  for (int i = 0; i < 4; i++)
#pragma unroll
    for (int j = 0; j < 2; j++) acc[i][j] = (f32x4){0.f, 0.f, 0.f, 0.f};
  const int c = tid & 7, r = tid >> 3;
  u32x4 areg[4], breg[2];
  float bmu[2], brs[2];
  auto gload = [&](int kt) {
#pragma unroll
    for (int p = 0; p < 4; p++) areg[p] = *(const u32x4*)(wsp + (size_t)(m0 + r + 32 * p) * S + kt * 64 + 8 * c);
#pragma unroll
    for (int p = 0; p < 2; p++) {
      const int kr = kt * 64 + r + 32 * p;
      breg[p] = *(const u32x4*)(z + (size_t)kr * F + FH + n0 + 8 * c);
      const float s1 = zsum[2 * kr], s2 = zsum[2 * kr + 1];
      const float mean = s1 * (1.f / FH);
      const float var = rb(fmaxf(s2 * (1.f / FH) - mean * mean, 0.f));
      bmu[p] = rb(mean);
      brs[p] = rb(sqrtf(rb(var + 1e-5f)));
    }
  };
  gload(0);
  for (int kt = 0; kt < 32; kt++) {
    __syncthreads();
#pragma unroll
    for (int p = 0; p < 4; p++) *(u32x4*)&As[(r + 32 * p) * 72 + 8 * c] = areg[p];
#pragma unroll
    for (int p = 0; p < 2; p++) {
      u16 t[8];
      *(u32x4*)t = breg[p];
#pragma unroll
      for (int e = 0; e < 8; e++) t[e] = f2bf(rb(bf2f(t[e]) - bmu[p]) / brs[p]);
      *(u32x4*)&Bs[(r + 32 * p) * 72 + 8 * c] = *(u32x4*)t;
    }
    __syncthreads();
    if (kt + 1 < 32) gload(kt + 1);
#pragma unroll
    for (int ks = 0; ks < 2; ks++) {
      s16x8 af[4], bfr[2];
#pragma unroll
      for (int i = 0; i < 4; i++)
        af[i] = *(const s16x8*)&As[(wr * 64 + i * 16 + (lane & 15)) * 72 + ks * 32 + (lane >> 4) * 8];
#pragma unroll
      for (int j = 0; j < 2; j++) bfr[j] = tr_frag(&Bs[(ks * 32) * 72 + wc * 32 + j * 16], 72, lane);
#pragma unroll
      for (int i = 0; i < 4; i++)
#pragma unroll
        for (int j = 0; j < 2; j++) acc[i][j] = __builtin_amdgcn_mfma_f32_16x16x32_bf16(af[i], bfr[j], acc[i][j], 0, 0, 0);
    }
  }
  const int fr = lane & 15, fq = lane >> 4;
#pragma unroll
  for (int i = 0; i < 4; i++)
#pragma unroll
    for (int reg = 0; reg < 4; reg++) {
      const int row = m0 + wr * 64 + i * 16 + fq * 4 + reg;
      const float bb = bf2f(bsp[row]);
#pragma unroll
      for (int j = 0; j < 2; j++) {
        const int col = n0 + wc * 32 + j * 16 + fr;
        const float g = rb(rb(acc[i][j][reg]) + bb);
        u[(size_t)row * FH + col] = f2bf(bf2f(z[(size_t)row * F + col]) * g);
      }
    }
}

__global__ __launch_bounds__(256) void k5_gemm3(const u16* __restrict__ u, const u16* __restrict__ wout,
                                                const u16* __restrict__ x1T, const float* __restrict__ modf,
                                                u16* __restrict__ out) {
  __shared__ __attribute__((aligned(16))) u16 As[64 * 72];
  __shared__ __attribute__((aligned(16))) u16 Bs[64 * 72];
  const int tid = threadIdx.x, wave = tid >> 6, lane = tid & 63;
  const int m0 = (blockIdx.x & 31) * 64, n0 = (blockIdx.x >> 5) * 64;
  const int wr = wave >> 1, wc = wave & 1;
  f32x4 acc[2][2];
#pragma unroll
  for (int i = 0; i < 2; i++)
#pragma unroll
    for (int j = 0; j < 2; j++) acc[i][j] = (f32x4){0.f, 0.f, 0.f, 0.f};
  const int c = tid & 7, r = tid >> 3;
  u32x4 areg[2], breg[2];
  auto gload = [&](int kt) {
#pragma unroll
    for (int p = 0; p < 2; p++) {
      areg[p] = *(const u32x4*)(u + (size_t)(m0 + r + 32 * p) * FH + kt * 64 + 8 * c);
      breg[p] = *(const u32x4*)(wout + (size_t)(kt * 64 + r + 32 * p) * D + n0 + 8 * c);
    }
  };
  gload(0);
  for (int kt = 0; kt < 14; kt++) {
    __syncthreads();
#pragma unroll
    for (int p = 0; p < 2; p++) {
      *(u32x4*)&As[(r + 32 * p) * 72 + 8 * c] = areg[p];
      *(u32x4*)&Bs[(r + 32 * p) * 72 + 8 * c] = breg[p];
    }
    __syncthreads();
    if (kt + 1 < 14) gload(kt + 1);
#pragma unroll
    for (int ks = 0; ks < 2; ks++) {
      s16x8 af[2], bfr[2];
#pragma unroll
      for (int i = 0; i < 2; i++)
        af[i] = *(const s16x8*)&As[(wr * 32 + i * 16 + (lane & 15)) * 72 + ks * 32 + (lane >> 4) * 8];
#pragma unroll
      for (int j = 0; j < 2; j++) bfr[j] = tr_frag(&Bs[(ks * 32) * 72 + wc * 32 + j * 16], 72, lane);
#pragma unroll
      for (int i = 0; i < 2; i++)
#pragma unroll
        for (int j = 0; j < 2; j++) acc[i][j] = __builtin_amdgcn_mfma_f32_16x16x32_bf16(af[i], bfr[j], acc[i][j], 0, 0, 0);
    }
  }
  const int fr = lane & 15, fq = lane >> 4;
#pragma unroll
  for (int i = 0; i < 2; i++)
#pragma unroll
    for (int j = 0; j < 2; j++) {
      const int col = n0 + wc * 32 + j * 16 + fr;
      const int row0 = m0 + wr * 32 + i * 16 + fq * 4;
      const float gb = modf[5 * D + col];
      u16 xv[4];
      *(uint2*)xv = *(const uint2*)(x1T + (size_t)col * S + row0);
#pragma unroll
      for (int reg = 0; reg < 4; reg++)
        out[(size_t)(row0 + reg) * D + col] = f2bf(bf2f(xv[reg]) + rb(gb * rb(acc[i][j][reg])));
    }
}

extern "C" void kernel_launch(void* const* d_in, const int* in_sizes, int n_in, void* d_out, int out_size,
                              void* d_ws, size_t ws_size, hipStream_t stream) {
  const u16* q = (const u16*)d_in[0];
  const u16* k = (const u16*)d_in[1];
  const u16* v = (const u16*)d_in[2];
  const u16* shk = (const u16*)d_in[3];
  const u16* ssmk = (const u16*)d_in[4];
  const u16* ssmD = (const u16*)d_in[5];
  const u16* win = (const u16*)d_in[7];
  const u16* wsp = (const u16*)d_in[8];
  const u16* bsp = (const u16*)d_in[9];
  const u16* wout = (const u16*)d_in[10];
  const u16* cvec = (const u16*)d_in[11];
  const u16* wmod = (const u16*)d_in[12];
  u16* out = (u16*)d_out;
  char* ws = (char*)d_ws;
  size_t off = 0;
  auto take = [&](size_t bytes) { char* p = ws + off; off += (bytes + 255) & ~(size_t)255; return p; };
  u16* kvT = (u16*)take((size_t)D * S * 2);
  u16* xT = (u16*)take((size_t)D * S * 2);
  u16* x1T = (u16*)take((size_t)D * S * 2);
  float* xstat = (float*)take((size_t)S * 2 * 4);
  float* modf = (float*)take((size_t)6 * D * 4);
  float* zsum = (float*)take((size_t)S * 2 * 4);
  u16* z = (u16*)take((size_t)S * F * 2);
  u16* u = (u16*)take((size_t)S * FH * 2);

  k1_prep<<<330, 256, 0, stream>>>(q, k, v, shk, cvec, wmod, kvT, xT, xstat, modf, zsum);
  k2_conv<<<D, 256, 0, stream>>>(kvT, xT, ssmk, ssmD, xstat, modf, x1T);
  k3_gemm1<<<32 * 14, 256, 0, stream>>>(x1T, win, modf, z, zsum);
  k4_gemm2<<<16 * 14, 256, 0, stream>>>(wsp, z, zsum, bsp, u);
  k5_gemm3<<<32 * 7, 256, 0, stream>>>(u, wout, x1T, modf, out);
}
```

```cpp
#include <hip/hip_runtime.h>
#include <hip/hip_bf16.h>
#include <stdint.h>

typedef unsigned short u16;
typedef short s16x8 __attribute__((ext_vector_type(8)));
typedef short s16x4 __attribute__((ext_vector_type(4)));
typedef float f32x4 __attribute__((ext_vector_type(4)));
typedef unsigned int u32x4 __attribute__((ext_vector_type(4)));
typedef __attribute__((address_space(3))) s16x4* lds_v4p;

constexpr int S = 2048, D = 448, F = 1792, FH = 896;

__device__ __forceinline__ float bf2f(u16 h) { return __uint_as_float(((unsigned)h) << 16); }
__device__ __forceinline__ u16 f2bf(float f) {
  unsigned u = __float_as_uint(f);
  u += 0x7fffu + ((u >> 16) & 1u);
  return (u16)(u >> 16);
}
__device__ __forceinline__ float rb(float f) { return bf2f(f2bf(f)); }

#define WAIT_V(n) asm volatile("s_waitcnt vmcnt(" #n ")" ::: "memory")
#define BAR() __builtin_amdgcn_s_barrier()

template <int ROWB>
__device__ __forceinline__ int swzB(int row) {
  if constexpr (ROWB == 128) return (((row >> 1) & 1) << 1) | (((row >> 3) & 1) << 2);
  else return ((row & 1) << 1) | (((row >> 1) & 1) << 2) | (((row >> 3) & 1) << 3);
}

struct TrF { s16x4 lo, hi; };
template <int ROWB>
__device__ __forceinline__ void tr_issue(TrF& f, const char* tile, int kbase, int nbase, int lane) {
  const int g = lane >> 4, q = (lane & 15) >> 2, p = lane & 3;
  const int n = nbase + 4 * p;
  const int r0 = kbase + 8 * g + q, r1 = r0 + 4;
  const unsigned a0 = (unsigned)(size_t)(tile + r0 * ROWB + (((n >> 3) ^ swzB<ROWB>(r0)) << 4) + ((n >> 2) & 1) * 8);
  const unsigned a1 = (unsigned)(size_t)(tile + r1 * ROWB + (((n >> 3) ^ swzB<ROWB>(r1)) << 4) + ((n >> 2) & 1) * 8);
  asm volatile("ds_read_b64_tr_b16 %0, %1" : "=v"(f.lo) : "v"(a0));
  asm volatile("ds_read_b64_tr_b16 %0, %1" : "=v"(f.hi) : "v"(a1));
}
__device__ __forceinline__ s16x8 tr_get(const TrF& f) {
  s16x8 r;
  r[0] = f.lo[0]; r[1] = f.lo[1]; r[2] = f.lo[2]; r[3] = f.lo[3];
  r[4] = f.hi[0]; r[5] = f.hi[1]; r[6] = f.hi[2]; r[7] = f.hi[3];
  return r;
}
#define TRW2(a, b) asm volatile("s_waitcnt lgkmcnt(0)" : "+v"(a.lo), "+v"(a.hi), "+v"(b.lo), "+v"(b.hi))
#define TRW4(a, b, c, d) asm volatile("s_waitcnt lgkmcnt(0)" : "+v"(a.lo), "+v"(a.hi), "+v"(b.lo), "+v"(b.hi), "+v"(c.lo), "+v"(c.hi), "+v"(d.lo), "+v"(d.hi))

__global__ __launch_bounds__(256) void k1_prep(const u16* __restrict__ q, const u16* __restrict__ k,
                                               const u16* __restrict__ v, const u16* __restrict__ shk,
                                               const u16* __restrict__ cvec, const u16* __restrict__ wmod,
                                               u16* __restrict__ kvT, u16* __restrict__ xT,
                                               float* __restrict__ xstat, float* __restrict__ modf,
                                               float* __restrict__ zsum) {
  __shared__ __attribute__((aligned(16))) char smem[2 * 64 * 72 * 2];
  const int b = blockIdx.x, tid = threadIdx.x;
  if (b < 224) {
    u16* Tkv = (u16*)smem;
    u16* Tx = Tkv + 64 * 72;
    const int st = b / 7, dt = b % 7;
    const int s0 = st * 64, d0 = dt * 64;
    const int c = tid & 7, r = tid >> 3;
    u16 sh[32];
    u16 qq[2][8], vv[2][8], kk[2][4][8];
    {
      const u32x4* sp = (const u32x4*)(shk + (size_t)(d0 + 8 * c) * 4);
#pragma unroll
      for (int i = 0; i < 4; i++) *(u32x4*)&sh[8 * i] = sp[i];
    }
#pragma unroll
    for (int pass = 0; pass < 2; pass++) {
      const int s = s0 + r + 32 * pass;
      *(u32x4*)qq[pass] = *(const u32x4*)(q + (size_t)s * D + d0 + 8 * c);
      *(u32x4*)vv[pass] = *(const u32x4*)(v + (size_t)s * D + d0 + 8 * c);
#pragma unroll
      for (int j = 0; j < 4; j++) {
        const int sj = s - j >= 0 ? s - j : 0;
        u32x4 t = *(const u32x4*)(k + (size_t)sj * D + d0 + 8 * c);
        if (s - j < 0) t = (u32x4){0, 0, 0, 0};
        *(u32x4*)kk[pass][j] = t;
      }
    }
#pragma unroll
    for (int pass = 0; pass < 2; pass++) {
      const int row = r + 32 * pass;
#pragma unroll
      for (int e = 0; e < 8; e++) {
        float acc = 0.f;
#pragma unroll
        for (int j = 3; j >= 0; j--) acc = rb(acc + rb(bf2f(kk[pass][j][e]) * bf2f(sh[e * 4 + j])));
        Tkv[(8 * c + e) * 72 + row] = f2bf(acc * bf2f(vv[pass][e]));
        Tx[(8 * c + e) * 72 + row] = qq[pass][e];
      }
    }
    __syncthreads();
#pragma unroll
    for (int pass = 0; pass < 2; pass++) {
      const int dr = r + 32 * pass;
      *(u32x4*)(kvT + (size_t)(d0 + dr) * S + s0 + 8 * c) = *(const u32x4*)(Tkv + dr * 72 + 8 * c);
      *(u32x4*)(xT + (size_t)(d0 + dr) * S + s0 + 8 * c) = *(const u32x4*)(Tx + dr * 72 + 8 * c);
    }
  } else if (b < 736) {
    const int wave = tid >> 6, lane = tid & 63;
    const int s = (b - 224) * 4 + wave;
    float xv[8];
    float sum = 0.f;
    if (lane < 56) {
      u16 t[8];
      *(u32x4*)t = *(const u32x4*)(q + (size_t)s * D + lane * 8);
#pragma unroll
      for (int e = 0; e < 8; e++) { xv[e] = bf2f(t[e]); sum += xv[e]; }
    } else {
#pragma unroll
      for (int e = 0; e < 8; e++) xv[e] = 0.f;
    }
#pragma unroll
    for (int o = 32; o >= 1; o >>= 1) sum += __shfl_xor(sum, o);
    const float mean = sum * (1.f / D);
    float sq = 0.f;
    if (lane < 56) {
#pragma unroll
      for (int e = 0; e < 8; e++) { float dd = xv[e] - mean; sq += dd * dd; }
    }
#pragma unroll
    for (int o = 32; o >= 1; o >>= 1) sq += __shfl_xor(sq, o);
    if (lane == 0) {
      const float var = rb(sq * (1.f / D));
      xstat[2 * s] = rb(mean);
      xstat[2 * s + 1] = rb(sqrtf(rb(var + 1e-5f)));
    }
    if (lane == 2 || lane == 3) zsum[2 * s + lane - 2] = 0.f;
  } else {
    float* red = (float*)smem;
    const int n0 = (b - 736) * 32;
    const int c4 = tid & 3, ks = tid >> 2;
    u32x4 t[7];
    float cv[7];
#pragma unroll
    for (int kk = 0; kk < 7; kk++) {
      const int kr = ks * 7 + kk;
      t[kk] = *(const u32x4*)(wmod + (size_t)kr * (6 * D) + n0 + 8 * c4);
      cv[kk] = bf2f(cvec[kr]);
    }
    float acc[8];
#pragma unroll
    for (int e = 0; e < 8; e++) acc[e] = 0.f;
#pragma unroll
    for (int kk = 0; kk < 7; kk++) {
      const float sc = rb(cv[kk] / (1.f + __expf(-cv[kk])));
      u16 tt[8];
      *(u32x4*)tt = t[kk];
#pragma unroll
      for (int e = 0; e < 8; e++) acc[e] += sc * bf2f(tt[e]);
    }
#pragma unroll
    for (int e = 0; e < 8; e++) red[ks * 32 + c4 * 8 + e] = acc[e];
    __syncthreads();
    if (tid < 32) {
      float sum = 0.f;
#pragma unroll
      for (int i = 0; i < 64; i++) sum += red[i * 32 + tid];
      modf[n0 + tid] = rb(sum);
    }
  }
}

__global__ __launch_bounds__(256) void k2_conv(const u16* __restrict__ kvT, const u16* __restrict__ xT,
                                               const u16* __restrict__ ssmk, const u16* __restrict__ ssmD,
                                               const float* __restrict__ xstat, const float* __restrict__ modf,
                                               u16* __restrict__ x1T) {
  __shared__ __attribute__((aligned(16))) u16 KFs[2208];
  __shared__ __attribute__((aligned(16))) u16 Rs[4112];
  __shared__ __attribute__((aligned(16))) float ybuf[4][2112];
  const int d = blockIdx.x, tid = threadIdx.x;
  const int wave = tid >> 6, lane = tid & 63;
  u16 kvv[8], xv[8];
  f32x4 st[4];
  const u32x4 kf = *(const u32x4*)(ssmk + (size_t)d * S + 8 * tid);
  *(u32x4*)kvv = *(const u32x4*)(kvT + (size_t)d * S + 8 * tid);
  *(u32x4*)xv = *(const u32x4*)(xT + (size_t)d * S + 8 * tid);
#pragma unroll
  for (int i = 0; i < 4; i++) st[i] = *(const f32x4*)(xstat + 16 * tid + 4 * i);
  const float Dd = bf2f(ssmD[d]);
  const float sa = modf[d], opca = rb(1.f + modf[D + d]), ga = modf[2 * D + d];
  {
    u32x4 z = {0, 0, 0, 0};
    if (tid < 16) *(u32x4*)&KFs[8 * tid] = z;
    if (tid < 4) *(u32x4*)&KFs[2176 + 8 * tid] = z;
    *(u32x4*)&KFs[128 + 8 * tid] = kf;
    u16 tr[8];
#pragma unroll
    for (int e = 0; e < 8; e++) tr[7 - e] = kvv[e];
    *(u32x4*)&Rs[2040 - 8 * tid + 8] = *(u32x4*)tr;
    if (tid == 0) *(u32x4*)&Rs[0] = z;
    *(u32x4*)&Rs[2056 + 8 * tid] = z;
    if (tid == 0) *(u32x4*)&Rs[2056 + 8 * 256] = z;
  }
  __syncthreads();
  const int m = lane & 15, h = lane >> 4;
  f32x4 acc[8];
#pragma unroll
  for (int p = 0; p < 8; p++) acc[p] = (f32x4){0.f, 0.f, 0.f, 0.f};
  const int tbeg = wave == 0 ? 0 : 17 + 16 * (wave - 1);
  const int tend = wave == 0 ? 17 : tbeg + 16;
#pragma unroll 2
  for (int t = tbeg; t < tend; t++) {
    const int q0 = -128 + 32 * t;
    const s16x8 a = *(const s16x8*)&KFs[8 * m + q0 + 8 * h + 128];
    const int basep = 2040 - 128 * m + q0 + 8 * h;
    const u32x4 w0 = *(const u32x4*)&Rs[basep + 8];
    const u32x4 w1 = *(const u32x4*)&Rs[basep + 16];
    unsigned W[8] = {w0[0], w0[1], w0[2], w0[3], w1[0], w1[1], w1[2], w1[3]};
#pragma unroll
    for (int p = 0; p < 8; p++) {
      const int sft = 7 - p;
      u32x4 fr;
      if ((sft & 1) == 0) {
#pragma unroll
        for (int i = 0; i < 4; i++) fr[i] = W[sft / 2 + i];
      } else {
#pragma unroll
        for (int i = 0; i < 4; i++) fr[i] = __builtin_amdgcn_alignbit(W[(sft - 1) / 2 + i + 1], W[(sft - 1) / 2 + i], 16);
      }
      acc[p] = __builtin_amdgcn_mfma_f32_16x16x32_bf16(a, *(s16x8*)&fr, acc[p], 0, 0, 0);
    }
  }
  {
    const int i = lane & 15;
#pragma unroll
    for (int reg = 0; reg < 4; reg++) {
      const int mm = 4 * h + reg;
      const int s = 128 * i + 8 * mm;
      f32x4 v0 = {acc[0][reg], acc[1][reg], acc[2][reg], acc[3][reg]};
      f32x4 v1 = {acc[4][reg], acc[5][reg], acc[6][reg], acc[7][reg]};
      *(f32x4*)&ybuf[wave][s + 4 * i] = v0;
      *(f32x4*)&ybuf[wave][s + 4 * i + 4] = v1;
    }
  }
  __syncthreads();
  {
    const int s0 = 8 * tid;
    const int idx = s0 + 4 * (s0 >> 7);
    float y[8];
#pragma unroll
    for (int e = 0; e < 8; e++) y[e] = 0.f;
#pragma unroll
    for (int w = 0; w < 4; w++) {
      f32x4 v0 = *(const f32x4*)&ybuf[w][idx];
      f32x4 v1 = *(const f32x4*)&ybuf[w][idx + 4];
#pragma unroll
      for (int e = 0; e < 4; e++) { y[e] += v0[e]; y[4 + e] += v1[e]; }
    }
    u16 o[8];
#pragma unroll
    for (int e = 0; e < 8; e++) {
      const float mean = st[e >> 1][(e & 1) * 2], sd = st[e >> 1][(e & 1) * 2 + 1];
      const float yy = rb(y[e]);
      const float y2 = rb(yy + rb(bf2f(kvv[e]) * Dd));
      const float x = bf2f(xv[e]);
      const float ln = rb(rb(x - mean) / sd);
      const float hh = rb(rb(ln * opca) + sa);
      const float a = rb(hh * y2);
      o[e] = f2bf(x + rb(ga * a));
    }
    *(u32x4*)(x1T + (size_t)d * S + s0) = *(u32x4*)o;
  }
}

template <int NI>
__device__ __forceinline__ void stage_kc(const u16* __restrict__ g, int ld, char* lds, int tid) {
#pragma unroll
  for (int i = 0; i < NI; i++) {
    const int b = i * 4096 + tid * 16;
    const int row = b >> 7, ch = (b >> 4) & 7;
    const int sch = ch ^ (row & 7);
    __builtin_amdgcn_global_load_lds((const unsigned*)(g + (size_t)row * ld + sch * 8), (unsigned*)(lds + b), 16, 0, 0);
  }
}
template <int ROWB, int NI>
__device__ __forceinline__ void stage_kn(const u16* __restrict__ g, int ld, char* lds, int tid) {
#pragma unroll
  for (int i = 0; i < NI; i++) {
    const int b = i * 4096 + tid * 16;
    const int row = b / ROWB, ch = (b % ROWB) >> 4;
    const int sch = ch ^ swzB<ROWB>(row);
    __builtin_amdgcn_global_load_lds((const unsigned*)(g + (size_t)row * ld + sch * 8), (unsigned*)(lds + b), 16, 0, 0);
  }
}

__device__ __forceinline__ float gelu_tanh(float x) {
  const float y = 0.7978845608028654f * (x + 0.044715f * x * x * x);
  const float t = 1.f - 2.f / (__expf(2.f * y) + 1.f);
  return 0.5f * x * (1.f + t);
}

constexpr int K3_A = 448 * 128;
constexpr int K3_BST = 64 * 512;
__global__ __launch_bounds__(256) void k3_gemm1(const u16* __restrict__ x1T, const u16* __restrict__ win,
                                                const float* __restrict__ modf, u16* __restrict__ z,
                                                float* __restrict__ zsum) {
  __shared__ __attribute__((aligned(16))) char smem[K3_A + 3 * K3_BST];
  char* As = smem;
  char* Bs = smem + K3_A;
  const int tid = threadIdx.x, wave = tid >> 6, lane = tid & 63;
  const int m0 = (blockIdx.x & 31) * 64, n0 = (blockIdx.x >> 5) * 256;
  stage_kn<512, 8>(win + n0, F, Bs, tid);
  stage_kn<512, 8>(win + (size_t)64 * F + n0, F, Bs + K3_BST, tid);
  {
    const int c = tid & 7, r = tid >> 3;
    u32x4 xr[14];
#pragma unroll
    for (int p = 0; p < 14; p++) xr[p] = *(const u32x4*)(x1T + (size_t)(r + 32 * p) * S + m0 + 8 * c);
    float s1[8], s2[8];
#pragma unroll
    for (int e = 0; e < 8; e++) { s1[e] = 0.f; s2[e] = 0.f; }
#pragma unroll
    for (int p = 0; p < 14; p++) {
      u16 t[8];
      *(u32x4*)t = xr[p];
#pragma unroll
      for (int e = 0; e < 8; e++) { float x = bf2f(t[e]); s1[e] += x; s2[e] += x * x; }
    }
#pragma unroll
    for (int e = 0; e < 8; e++) {
#pragma unroll
      for (int o = 8; o <= 32; o <<= 1) { s1[e] += __shfl_xor(s1[e], o); s2[e] += __shfl_xor(s2[e], o); }
    }
    float* red = (float*)(Bs + 2 * K3_BST);
    if (lane < 8) {
#pragma unroll
      for (int e = 0; e < 8; e++) { red[wave * 128 + (c * 8 + e) * 2] = s1[e]; red[wave * 128 + (c * 8 + e) * 2 + 1] = s2[e]; }
    }
    __syncthreads();
    float mu[8], rs[8];
#pragma unroll
    for (int e = 0; e < 8; e++) {
      const int col = c * 8 + e;
      const float a1 = red[col * 2] + red[128 + col * 2] + red[256 + col * 2] + red[384 + col * 2];
      const float a2 = red[col * 2 + 1] + red[128 + col * 2 + 1] + red[256 + col * 2 + 1] + red[384 + col * 2 + 1];
      const float mean = a1 * (1.f / D);
      const float var = rb(fmaxf(a2 * (1.f / D) - mean * mean, 0.f));
      mu[e] = rb(mean);
      rs[e] = rb(sqrtf(rb(var + 1e-5f)));
    }
#pragma unroll
    for (int p = 0; p < 14; p++) {
      const int kr = r + 32 * p;
      const float opc = rb(1.f + modf[4 * D + kr]), sb = modf[3 * D + kr];
      u16 t[8];
      *(u32x4*)t = xr[p];
#pragma unroll
      for (int e = 0; e < 8; e++) {
        const float ln = rb(rb(bf2f(t[e]) - mu[e]) / rs[e]);
        t[e] = f2bf(rb(ln * opc) + sb);
      }
      *(u32x4*)(As + kr * 128 + ((c ^ swzB<128>(kr)) << 4)) = *(u32x4*)t;
    }
  }
  const int wr = wave >> 1, wc = wave & 1;
  f32x4 acc[2][8];
#pragma unroll
  for (int i = 0; i < 2; i++)
#pragma unroll
    for (int j = 0; j < 8; j++) acc[i][j] = (f32x4){0.f, 0.f, 0.f, 0.f};
  for (int kt = 0; kt < 7; kt++) {
    if (kt < 6) { WAIT_V(8); } else { WAIT_V(0); }
    asm volatile("s_waitcnt lgkmcnt(0)" ::: "memory");
    BAR();
    if (kt + 2 < 7) stage_kn<512, 8>(win + (size_t)(kt + 2) * 64 * F + n0, F, Bs + ((kt + 2) % 3) * K3_BST, tid);
    const char* Bt = Bs + (kt % 3) * K3_BST;
#pragma unroll
    for (int ks = 0; ks < 2; ks++) {
      s16x8 af[2], bfr[8];
      TrF ta[2], tb[8];
#pragma unroll
      for (int i = 0; i < 2; i++) tr_issue<128>(ta[i], As, kt * 64 + ks * 32, wr * 32 + i * 16, lane);
#pragma unroll
      for (int j = 0; j < 8; j++) tr_issue<512>(tb[j], Bt, ks * 32, wc * 128 + j * 16, lane);
      TRW2(ta[0], ta[1]); TRW4(tb[0], tb[1], tb[2], tb[3]); TRW4(tb[4], tb[5], tb[6], tb[7]);
#pragma unroll
      for (int i = 0; i < 2; i++) af[i] = tr_get(ta[i]);
#pragma unroll
      for (int j = 0; j < 8; j++) bfr[j] = tr_get(tb[j]);
#pragma unroll
      for (int i = 0; i < 2; i++)
#pragma unroll
        for (int j = 0; j < 8; j++) acc[i][j] = __builtin_amdgcn_mfma_f32_16x16x32_bf16(af[i], bfr[j], acc[i][j], 0, 0, 0);
    }
  }
  __syncthreads();
  u16* Ct = (u16*)smem;
  const int fr = lane & 15, fq = lane >> 4;
#pragma unroll
  for (int i = 0; i < 2; i++) {
#pragma unroll
    for (int reg = 0; reg < 4; reg++) {
      const int row = wr * 32 + i * 16 + fq * 4 + reg;
      float s1 = 0.f, s2 = 0.f;
#pragma unroll
      for (int j = 0; j < 8; j++) {
        const float g = rb(gelu_tanh(rb(acc[i][j][reg])));
        Ct[row * 264 + wc * 128 + j * 16 + fr] = f2bf(g);
        s1 += g; s2 += g * g;
      }
      if (n0 + 256 > FH) {
        const int cbase = n0 + wc * 128;
        if (cbase >= FH) {
#pragma unroll
          for (int o = 1; o < 16; o <<= 1) { s1 += __shfl_xor(s1, o); s2 += __shfl_xor(s2, o); }
          if (fr == 0) { atomicAdd(&zsum[2 * (m0 + row)], s1); atomicAdd(&zsum[2 * (m0 + row) + 1], s2); }
        }
      }
    }
  }
  __syncthreads();
#pragma unroll
  for (int p = 0; p < 8; p++) {
    const int idx = p * 256 + tid;
    const int row = idx >> 5, ch = idx & 31;
    *(u32x4*)(z + (size_t)(m0 + row) * F + n0 + ch * 8) = *(const u32x4*)(Ct + row * 264 + ch * 8);
  }
}

__global__ __launch_bounds__(256) void k3b_norm(const u16* __restrict__ z, const float* __restrict__ zsum,
                                                u16* __restrict__ z2n) {
  const int idx = blockIdx.x * 256 + threadIdx.x;
  const int row = idx / 112, ch = idx % 112;
  u16 t[8];
  *(u32x4*)t = *(const u32x4*)(z + (size_t)row * F + FH + ch * 8);
  const float s1 = zsum[2 * row], s2 = zsum[2 * row + 1];
  const float mean = s1 * (1.f / FH);
  const float var = rb(fmaxf(s2 * (1.f / FH) - mean * mean, 0.f));
  const float mu = rb(mean), sd = rb(sqrtf(rb(var + 1e-5f)));
#pragma unroll
  for (int e = 0; e < 8; e++) t[e] = f2bf(rb(bf2f(t[e]) - mu) / sd);
  *(u32x4*)(z2n + (size_t)row * FH + ch * 8) = *(u32x4*)t;
}

constexpr int K4_ST = 128 * 128 + 64 * 128;
__global__ __launch_bounds__(256) void k4_gemm2(const u16* __restrict__ wsp, const u16* __restrict__ z2n,
                                                const u16* __restrict__ z, const u16* __restrict__ bsp,
                                                u16* __restrict__ u) {
  __shared__ __attribute__((aligned(16))) char smem[4 * K4_ST];
  const int tid = threadIdx.x, wave = tid >> 6, lane = tid & 63;
  const int b = blockIdx.x;
  const int xcd = b & 7, idx = b >> 3;
  const int mt = xcd * 2 + (idx & 1), nt = idx >> 1;
  const int m0 = mt * 128, n0 = nt * 64;
  const int wr = wave >> 1, wc = wave & 1;
  f32x4 acc[4][2];
#pragma unroll
  for (int i = 0; i < 4; i++)
#pragma unroll
    for (int j = 0; j < 2; j++) acc[i][j] = (f32x4){0.f, 0.f, 0.f, 0.f};
  auto issue = [&](int kt) {
    char* st = smem + (kt & 3) * K4_ST;
    stage_kc<4>(wsp + (size_t)m0 * S + kt * 64, S, st, tid);
    stage_kn<128, 2>(z2n + (size_t)(kt * 64) * FH + n0, FH, st + 128 * 128, tid);
  };
  issue(0); issue(1); issue(2);
  for (int kt = 0; kt < 32; kt++) {
    if (kt < 30) { WAIT_V(12); } else if (kt == 30) { WAIT_V(6); } else { WAIT_V(0); }
    asm volatile("s_waitcnt lgkmcnt(0)" ::: "memory");
    BAR();
    if (kt + 3 < 32) issue(kt + 3);
    const char* At = smem + (kt & 3) * K4_ST;
    const char* Bt = At + 128 * 128;
#pragma unroll
    for (int ks = 0; ks < 2; ks++) {
      s16x8 af[4], bfr[2];
      TrF tb[2];
#pragma unroll
      for (int i = 0; i < 4; i++) {
        const int row = wr * 64 + i * 16 + (lane & 15);
        af[i] = *(const s16x8*)(At + row * 128 + (((ks * 4 + (lane >> 4)) ^ (row & 7)) << 4));
      }
#pragma unroll
      for (int j = 0; j < 2; j++) tr_issue<128>(tb[j], Bt, ks * 32, wc * 32 + j * 16, lane);
      TRW2(tb[0], tb[1]);
      bfr[0] = tr_get(tb[0]); bfr[1] = tr_get(tb[1]);
#pragma unroll
      for (int i = 0; i < 4; i++)
#pragma unroll
        for (int j = 0; j < 2; j++) acc[i][j] = __builtin_amdgcn_mfma_f32_16x16x32_bf16(af[i], bfr[j], acc[i][j], 0, 0, 0);
    }
  }
  __syncthreads();
  float* Ct = (float*)smem;
  const int fr = lane & 15, fq = lane >> 4;
#pragma unroll
  for (int i = 0; i < 4; i++)
#pragma unroll
    for (int reg = 0; reg < 4; reg++) {
      const int row = wr * 64 + i * 16 + fq * 4 + reg;
#pragma unroll
      for (int j = 0; j < 2; j++) Ct[row * 68 + wc * 32 + j * 16 + fr] = acc[i][j][reg];
    }
  __syncthreads();
#pragma unroll
  for (int p = 0; p < 4; p++) {
    const int id = p * 256 + tid;
    const int row = id >> 3, ch = id & 7;
    const float bb = bf2f(bsp[m0 + row]);
    u16 t[8];
    *(u32x4*)t = *(const u32x4*)(z + (size_t)(m0 + row) * F + n0 + ch * 8);
#pragma unroll
    for (int e = 0; e < 8; e++) {
      const float g = rb(rb(Ct[row * 68 + ch * 8 + e]) + bb);
      t[e] = f2bf(bf2f(t[e]) * g);
    }
    *(u32x4*)(u + (size_t)(m0 + row) * FH + n0 + ch * 8) = *(u32x4*)t;
  }
}

constexpr int K5_ST = 64 * 128 + 64 * 128;
__global__ __launch_bounds__(256) void k5_gemm3(const u16* __restrict__ u, const u16* __restrict__ wout,
                                                const u16* __restrict__ x1T, const float* __restrict__ modf,
                                                u16* __restrict__ out) {
  __shared__ __attribute__((aligned(16))) char smem[4 * K5_ST];
  const int tid = threadIdx.x, wave = tid >> 6, lane = tid & 63;
  const int b = blockIdx.x;
  const int xcd = b & 7, idx = b >> 3;
  const int mt = xcd * 4 + (idx & 3), nt = idx >> 2;
  const int m0 = mt * 64, n0 = nt * 64;
  const int wr = wave >> 1, wc = wave & 1;
  f32x4 acc[2][2];
#pragma unroll
  for (int i = 0; i < 2; i++)
#pragma unroll
    for (int j = 0; j < 2; j++) acc[i][j] = (f32x4){0.f, 0.f, 0.f, 0.f};
  auto issue = [&](int kt) {
    char* st = smem + (kt & 3) * K5_ST;
    stage_kc<2>(u + (size_t)m0 * FH + kt * 64, FH, st, tid);
    stage_kn<128, 2>(wout + (size_t)(kt * 64) * D + n0, D, st + 64 * 128, tid);
  };
  issue(0); issue(1); issue(2);
  for (int kt = 0; kt < 14; kt++) {
    if (kt < 12) { WAIT_V(8); } else if (kt == 12) { WAIT_V(4); } else { WAIT_V(0); }
    asm volatile("s_waitcnt lgkmcnt(0)" ::: "memory");
    BAR();
    if (kt + 3 < 14) issue(kt + 3);
    const char* At = smem + (kt & 3) * K5_ST;
    const char* Bt = At + 64 * 128;
#pragma unroll
    for (int ks = 0; ks < 2; ks++) {
      s16x8 af[2], bfr[2];
      TrF tb[2];
#pragma unroll
      for (int i = 0; i < 2; i++) {
        const int row = wr * 32 + i * 16 + (lane & 15);
        af[i] = *(const s16x8*)(At + row * 128 + (((ks * 4 + (lane >> 4)) ^ (row & 7)) << 4));
      }
#pragma unroll
      for (int j = 0; j < 2; j++) tr_issue<128>(tb[j], Bt, ks * 32, wc * 32 + j * 16, lane);
      TRW2(tb[0], tb[1]);
      bfr[0] = tr_get(tb[0]); bfr[1] = tr_get(tb[1]);
#pragma unroll
      for (int i = 0; i < 2; i++)
#pragma unroll
        for (int j = 0; j < 2; j++) acc[i][j] = __builtin_amdgcn_mfma_f32_16x16x32_bf16(af[i], bfr[j], acc[i][j], 0, 0, 0);
    }
  }
  __syncthreads();
  u16* Ct = (u16*)smem;
  const int fr = lane & 15, fq = lane >> 4;
#pragma unroll
  for (int i = 0; i < 2; i++)
#pragma unroll
    for (int j = 0; j < 2; j++) {
      const int col = wc * 32 + j * 16 + fr;
      const int row0 = wr * 32 + i * 16 + fq * 4;
      const float gb = modf[5 * D + n0 + col];
      u16 xv[4];
      *(uint2*)xv = *(const uint2*)(x1T + (size_t)(n0 + col) * S + m0 + row0);
#pragma unroll
      for (int reg = 0; reg < 4; reg++) Ct[(row0 + reg) * 72 + col] = f2bf(bf2f(xv[reg]) + rb(gb * rb(acc[i][j][reg])));
    }
  __syncthreads();
#pragma unroll
  for (int p = 0; p < 2; p++) {
    const int id = p * 256 + tid;
    const int row = id >> 3, ch = id & 7;
    *(u32x4*)(out + (size_t)(m0 + row) * D + n0 + ch * 8) = *(const u32x4*)(Ct + row * 72 + ch * 8);
  }
}

extern "C" void kernel_launch(void* const* d_in, const int* in_sizes, int n_in, void* d_out, int out_size,
                              void* d_ws, size_t ws_size, hipStream_t stream) {
  const u16* q = (const u16*)d_in[0];
  const u16* k = (const u16*)d_in[1];
  const u16* v = (const u16*)d_in[2];
  const u16* shk = (const u16*)d_in[3];
  const u16* ssmk = (const u16*)d_in[4];
  const u16* ssmD = (const u16*)d_in[5];
  const u16* win = (const u16*)d_in[7];
  const u16* wsp = (const u16*)d_in[8];
  const u16* bsp = (const u16*)d_in[9];
  const u16* wout = (const u16*)d_in[10];
  const u16* cvec = (const u16*)d_in[11];
  const u16* wmod = (const u16*)d_in[12];
  u16* out = (u16*)d_out;
  char* ws = (char*)d_ws;
  size_t off = 0;
  auto take = [&](size_t bytes) { char* p = ws + off; off += (bytes + 255) & ~(size_t)255; return p; };
  u16* kvT = (u16*)take((size_t)D * S * 2);
  u16* xT = (u16*)take((size_t)D * S * 2);
  u16* x1T = (u16*)take((size_t)D * S * 2);
  float* xstat = (float*)take((size_t)S * 2 * 4);
  float* modf = (float*)take((size_t)6 * D * 4);
  float* zsum = (float*)take((size_t)S * 2 * 4);
  u16* z = (u16*)take((size_t)S * F * 2);
  u16* z2n = (u16*)take((size_t)S * FH * 2);
  u16* u = (u16*)take((size_t)S * FH * 2);

  k1_prep<<<820, 256, 0, stream>>>(q, k, v, shk, cvec, wmod, kvT, xT, xstat, modf, zsum);
  k2_conv<<<D, 256, 0, stream>>>(kvT, xT, ssmk, ssmD, xstat, modf, x1T);
  k3_gemm1<<<32 * 7, 256, 0, stream>>>(x1T, win, modf, z, zsum);
  k3b_norm<<<896, 256, 0, stream>>>(z, zsum, z2n);
  k4_gemm2<<<16 * 14, 256, 0, stream>>>(wsp, z2n, z, bsp, u);
  k5_gemm3<<<32 * 7, 256, 0, stream>>>(u, wout, x1T, modf, out);
}
```

```cpp
#include <hip/hip_runtime.h>
#include <hip/hip_bf16.h>
#include <stdint.h>

typedef unsigned short u16;
typedef short s16x8 __attribute__((ext_vector_type(8)));
typedef short s16x4 __attribute__((ext_vector_type(4)));
typedef float f32x4 __attribute__((ext_vector_type(4)));
typedef unsigned int u32x4 __attribute__((ext_vector_type(4)));
typedef __attribute__((address_space(3))) s16x4* lds_v4p;

constexpr int S = 2048, D = 448, F = 1792, FH = 896;

__device__ __forceinline__ float bf2f(u16 h) { return __uint_as_float(((unsigned)h) << 16); }
__device__ __forceinline__ u16 f2bf(float f) {
  unsigned u = __float_as_uint(f);
  u += 0x7fffu + ((u >> 16) & 1u);
  return (u16)(u >> 16);
}
typedef __bf16 bf2v __attribute__((ext_vector_type(2)));
typedef float f2v __attribute__((ext_vector_type(2)));
__device__ __forceinline__ unsigned pk2(float a, float b) {
  f2v f = {a, b};
  bf2v r = __builtin_convertvector(f, bf2v);
  return __builtin_bit_cast(unsigned, r);
}
__device__ __forceinline__ float lo16(unsigned u) { return __uint_as_float(u << 16); }
__device__ __forceinline__ float hi16(unsigned u) { return __uint_as_float(u & 0xffff0000u); }
__device__ __forceinline__ float rb(float f) { return lo16(pk2(f, f)); }

#define WAIT_V(n) asm volatile("s_waitcnt vmcnt(" #n ")" ::: "memory")
#define BAR() __builtin_amdgcn_s_barrier()

template <int ROWB>
__device__ __forceinline__ int swzB(int row) {
  if constexpr (ROWB == 128) return (((row >> 1) & 1) << 1) | (((row >> 3) & 1) << 2);
  else return ((row & 1) << 1) | (((row >> 1) & 1) << 2) | (((row >> 3) & 1) << 3);
}

struct TrF { s16x4 lo, hi; };
template <int ROWB>
__device__ __forceinline__ void tr_issue(TrF& f, const char* tile, int kbase, int nbase, int lane) {
  const int g = lane >> 4, q = (lane & 15) >> 2, p = lane & 3;
  const int n = nbase + 4 * p;
  const int r0 = kbase + 8 * g + q, r1 = r0 + 4;
  const unsigned a0 = (unsigned)(size_t)(tile + r0 * ROWB + (((n >> 3) ^ swzB<ROWB>(r0)) << 4) + ((n >> 2) & 1) * 8);
  const unsigned a1 = (unsigned)(size_t)(tile + r1 * ROWB + (((n >> 3) ^ swzB<ROWB>(r1)) << 4) + ((n >> 2) & 1) * 8);
  asm volatile("ds_read_b64_tr_b16 %0, %1" : "=v"(f.lo) : "v"(a0));
  asm volatile("ds_read_b64_tr_b16 %0, %1" : "=v"(f.hi) : "v"(a1));
}
__device__ __forceinline__ s16x8 tr_get(const TrF& f) {
  s16x8 r;
  r[0] = f.lo[0]; r[1] = f.lo[1]; r[2] = f.lo[2]; r[3] = f.lo[3];
  r[4] = f.hi[0]; r[5] = f.hi[1]; r[6] = f.hi[2]; r[7] = f.hi[3];
  return r;
}
#define TRW2(a, b) asm volatile("s_waitcnt lgkmcnt(0)" : "+v"(a.lo), "+v"(a.hi), "+v"(b.lo), "+v"(b.hi))
#define TRW4(a, b, c, d) asm volatile("s_waitcnt lgkmcnt(0)" : "+v"(a.lo), "+v"(a.hi), "+v"(b.lo), "+v"(b.hi), "+v"(c.lo), "+v"(c.hi), "+v"(d.lo), "+v"(d.hi))

__global__ __launch_bounds__(256) void k1_prep(const u16* __restrict__ q, const u16* __restrict__ k,
                                               const u16* __restrict__ v, const u16* __restrict__ shk,
                                               const u16* __restrict__ cvec, const u16* __restrict__ wmod,
                                               u16* __restrict__ kvT, u16* __restrict__ xT,
                                               float* __restrict__ xstat, float* __restrict__ modf,
                                               float* __restrict__ zsum) {
  __shared__ __attribute__((aligned(16))) char smem[2 * 64 * 72 * 2];
  const int b = blockIdx.x, tid = threadIdx.x;
  if (b < 224) {
    u16* Tkv = (u16*)smem;
    u16* Tx = Tkv + 64 * 72;
    const int st = b / 7, dt = b % 7;
    const int s0 = st * 64, d0 = dt * 64;
    const int c = tid & 7, r = tid >> 3;
    u16 sh[32];
    u16 qq[2][8], vv[2][8], kk[2][4][8];
    {
      const u32x4* sp = (const u32x4*)(shk + (size_t)(d0 + 8 * c) * 4);
#pragma unroll
      for (int i = 0; i < 4; i++) *(u32x4*)&sh[8 * i] = sp[i];
    }
#pragma unroll
    for (int pass = 0; pass < 2; pass++) {
      const int s = s0 + r + 32 * pass;
      *(u32x4*)qq[pass] = *(const u32x4*)(q + (size_t)s * D + d0 + 8 * c);
      *(u32x4*)vv[pass] = *(const u32x4*)(v + (size_t)s * D + d0 + 8 * c);
#pragma unroll
      for (int j = 0; j < 4; j++) {
        const int sj = s - j >= 0 ? s - j : 0;
        u32x4 t = *(const u32x4*)(k + (size_t)sj * D + d0 + 8 * c);
        if (s - j < 0) t = (u32x4){0, 0, 0, 0};
        *(u32x4*)kk[pass][j] = t;
      }
    }
#pragma unroll
    for (int pass = 0; pass < 2; pass++) {
      const int row = r + 32 * pass;
#pragma unroll
      for (int e = 0; e < 8; e++) {
        float acc = 0.f;
#pragma unroll
        for (int j = 3; j >= 0; j--) acc = rb(acc + rb(bf2f(kk[pass][j][e]) * bf2f(sh[e * 4 + j])));
        Tkv[(8 * c + e) * 72 + row] = f2bf(acc * bf2f(vv[pass][e]));
        Tx[(8 * c + e) * 72 + row] = qq[pass][e];
      }
    }
    __syncthreads();
#pragma unroll
    for (int pass = 0; pass < 2; pass++) {
      const int dr = r + 32 * pass;
      *(u32x4*)(kvT + (size_t)(d0 + dr) * S + s0 + 8 * c) = *(const u32x4*)(Tkv + dr * 72 + 8 * c);
      *(u32x4*)(xT + (size_t)(d0 + dr) * S + s0 + 8 * c) = *(const u32x4*)(Tx + dr * 72 + 8 * c);
    }
  } else if (b < 736) {
    const int wave = tid >> 6, lane = tid & 63;
    const int s = (b - 224) * 4 + wave;
    float xv[8];
    float sum = 0.f;
    if (lane < 56) {
      u16 t[8];
      *(u32x4*)t = *(const u32x4*)(q + (size_t)s * D + lane * 8);
#pragma unroll
      for (int e = 0; e < 8; e++) { xv[e] = bf2f(t[e]); sum += xv[e]; }
    } else {
#pragma unroll
      for (int e = 0; e < 8; e++) xv[e] = 0.f;
    }
#pragma unroll
    for (int o = 32; o >= 1; o >>= 1) sum += __shfl_xor(sum, o);
    const float mean = sum * (1.f / D);
    float sq = 0.f;
    if (lane < 56) {
#pragma unroll
      for (int e = 0; e < 8; e++) { float dd = xv[e] - mean; sq += dd * dd; }
    }
#pragma unroll
    for (int o = 32; o >= 1; o >>= 1) sq += __shfl_xor(sq, o);
    if (lane == 0) {
      const float var = rb(sq * (1.f / D));
      xstat[2 * s] = rb(mean);
      xstat[2 * s + 1] = 1.f / rb(sqrtf(rb(var + 1e-5f)));
    }
    if (lane == 2 || lane == 3) zsum[2 * s + lane - 2] = 0.f;
  } else {
    float* red = (float*)smem;
    const int n0 = (b - 736) * 32;
    const int c4 = tid & 3, ks = tid >> 2;
    u32x4 t[7];
    float cv[7];
#pragma unroll
    for (int kk = 0; kk < 7; kk++) {
      const int kr = ks * 7 + kk;
      t[kk] = *(const u32x4*)(wmod + (size_t)kr * (6 * D) + n0 + 8 * c4);
      cv[kk] = bf2f(cvec[kr]);
    }
    float acc[8];
#pragma unroll
    for (int e = 0; e < 8; e++) acc[e] = 0.f;
#pragma unroll
    for (int kk = 0; kk < 7; kk++) {
      const float sc = rb(cv[kk] / (1.f + __expf(-cv[kk])));
      u16 tt[8];
      *(u32x4*)tt = t[kk];
#pragma unroll
      for (int e = 0; e < 8; e++) acc[e] += sc * bf2f(tt[e]);
    }
#pragma unroll
    for (int e = 0; e < 8; e++) red[ks * 32 + c4 * 8 + e] = acc[e];
    __syncthreads();
    if (tid < 32) {
      float sum = 0.f;
#pragma unroll
      for (int i = 0; i < 64; i++) sum += red[i * 32 + tid];
      modf[n0 + tid] = rb(sum);
    }
  }
}

#define RSK(i) ((i) + (((i) >> 7) << 3))
__global__ __launch_bounds__(256) void k2_conv(const u16* __restrict__ kvT, const u16* __restrict__ xT,
                                               const u16* __restrict__ ssmk, const u16* __restrict__ ssmD,
                                               const float* __restrict__ xstat, const float* __restrict__ modf,
                                               u16* __restrict__ x1T) {
  __shared__ __attribute__((aligned(16))) u16 KFs[2208];
  __shared__ __attribute__((aligned(16))) u16 Rs[4112 + 8 * 33];
  __shared__ __attribute__((aligned(16))) float ybuf[4][2112];
  const int d = blockIdx.x, tid = threadIdx.x;
  const int wave = tid >> 6, lane = tid & 63;
  u16 kvv[8], xv[8];
  f32x4 st[4];
  const u32x4 kf = *(const u32x4*)(ssmk + (size_t)d * S + 8 * tid);
  *(u32x4*)kvv = *(const u32x4*)(kvT + (size_t)d * S + 8 * tid);
  *(u32x4*)xv = *(const u32x4*)(xT + (size_t)d * S + 8 * tid);
#pragma unroll
  for (int i = 0; i < 4; i++) st[i] = *(const f32x4*)(xstat + 16 * tid + 4 * i);
  const float Dd = bf2f(ssmD[d]);
  const float sa = modf[d], opca = rb(1.f + modf[D + d]), ga = modf[2 * D + d];
  {
    u32x4 z = {0, 0, 0, 0};
    if (tid < 16) *(u32x4*)&KFs[8 * tid] = z;
    if (tid < 4) *(u32x4*)&KFs[2176 + 8 * tid] = z;
    *(u32x4*)&KFs[128 + 8 * tid] = kf;
    u16 tr[8];
#pragma unroll
    for (int e = 0; e < 8; e++) tr[7 - e] = kvv[e];
    *(u32x4*)&Rs[RSK(2040 - 8 * tid + 8)] = *(u32x4*)tr;
    if (tid == 0) *(u32x4*)&Rs[0] = z;
    *(u32x4*)&Rs[RSK(2056 + 8 * tid)] = z;
    if (tid == 0) *(u32x4*)&Rs[RSK(2056 + 8 * 256)] = z;
  }
  __syncthreads();
  const int m = lane & 15, h = lane >> 4;
  f32x4 acc[8];
#pragma unroll
  for (int p = 0; p < 8; p++) acc[p] = (f32x4){0.f, 0.f, 0.f, 0.f};
  const int tbeg = wave == 0 ? 0 : 17 + 16 * (wave - 1);
  const int tend = wave == 0 ? 17 : tbeg + 16;
#pragma unroll 2
  for (int t = tbeg; t < tend; t++) {
    const int q0 = -128 + 32 * t;
    const s16x8 a = *(const s16x8*)&KFs[8 * m + q0 + 8 * h + 128];
    const int basep = 2040 - 128 * m + q0 + 8 * h;
    const u32x4 w0 = *(const u32x4*)&Rs[RSK(basep + 8)];
    const u32x4 w1 = *(const u32x4*)&Rs[RSK(basep + 16)];
    unsigned W[8] = {w0[0], w0[1], w0[2], w0[3], w1[0], w1[1], w1[2], w1[3]};
#pragma unroll
    for (int p = 0; p < 8; p++) {
      const int sft = 7 - p;
      u32x4 fr;
      if ((sft & 1) == 0) {
#pragma unroll
        for (int i = 0; i < 4; i++) fr[i] = W[sft / 2 + i];
      } else {
#pragma unroll
        for (int i = 0; i < 4; i++) fr[i] = __builtin_amdgcn_alignbit(W[(sft - 1) / 2 + i + 1], W[(sft - 1) / 2 + i], 16);
      }
      acc[p] = __builtin_amdgcn_mfma_f32_16x16x32_bf16(a, *(s16x8*)&fr, acc[p], 0, 0, 0);
    }
  }
  {
    const int i = lane & 15;
#pragma unroll
    for (int reg = 0; reg < 4; reg++) {
      const int mm = 4 * h + reg;
      const int s = 128 * i + 8 * mm;
      f32x4 v0 = {acc[0][reg], acc[1][reg], acc[2][reg], acc[3][reg]};
      f32x4 v1 = {acc[4][reg], acc[5][reg], acc[6][reg], acc[7][reg]};
      *(f32x4*)&ybuf[wave][s + 4 * i] = v0;
      *(f32x4*)&ybuf[wave][s + 4 * i + 4] = v1;
    }
  }
  __syncthreads();
  {
    const int s0 = 8 * tid;
    const int idx = s0 + 4 * (s0 >> 7);
    float y[8];
#pragma unroll
    for (int e = 0; e < 8; e++) y[e] = 0.f;
#pragma unroll
    for (int w = 0; w < 4; w++) {
      f32x4 v0 = *(const f32x4*)&ybuf[w][idx];
      f32x4 v1 = *(const f32x4*)&ybuf[w][idx + 4];
#pragma unroll
      for (int e = 0; e < 4; e++) { y[e] += v0[e]; y[4 + e] += v1[e]; }
    }
    u16 o[8];
#pragma unroll
    for (int e = 0; e < 8; e++) {
      const float mean = st[e >> 1][(e & 1) * 2], sd = st[e >> 1][(e & 1) * 2 + 1];
      const float yy = rb(y[e]);
      const float y2 = rb(yy + rb(bf2f(kvv[e]) * Dd));
      const float x = bf2f(xv[e]);
      const float ln = rb(rb(x - mean) * sd);
      const float hh = rb(rb(ln * opca) + sa);
      const float a = rb(hh * y2);
      o[e] = f2bf(x + rb(ga * a));
    }
    *(u32x4*)(x1T + (size_t)d * S + s0) = *(u32x4*)o;
  }
}

template <int NI>
__device__ __forceinline__ void stage_kc(const u16* __restrict__ g, int ld, char* lds, int tid) {
#pragma unroll
  for (int i = 0; i < NI; i++) {
    const int b = i * 4096 + tid * 16;
    const int row = b >> 7, ch = (b >> 4) & 7;
    const int sch = ch ^ (row & 7);
    __builtin_amdgcn_global_load_lds((const unsigned*)(g + (size_t)row * ld + sch * 8), (unsigned*)(lds + b), 16, 0, 0);
  }
}
template <int ROWB, int NI>
__device__ __forceinline__ void stage_kn(const u16* __restrict__ g, int ld, char* lds, int tid) {
#pragma unroll
  for (int i = 0; i < NI; i++) {
    const int b = i * 4096 + tid * 16;
    const int row = b / ROWB, ch = (b % ROWB) >> 4;
    const int sch = ch ^ swzB<ROWB>(row);
    __builtin_amdgcn_global_load_lds((const unsigned*)(g + (size_t)row * ld + sch * 8), (unsigned*)(lds + b), 16, 0, 0);
  }
}

__device__ __forceinline__ float gelu_tanh(float x) {
  const float y = x * (2.302208198f + 0.1029432397f * x * x);
  const float e = __builtin_amdgcn_exp2f(-y);
  return x * __builtin_amdgcn_rcpf(1.f + e);
}

constexpr int K3_A = 448 * 128;
constexpr int K3_BST = 64 * 512;
__global__ __launch_bounds__(256) void k3_gemm1(const u16* __restrict__ x1T, const u16* __restrict__ win,
                                                const float* __restrict__ modf, u16* __restrict__ z,
                                                float* __restrict__ zsum) {
  __shared__ __attribute__((aligned(16))) char smem[K3_A + 3 * K3_BST];
  char* As = smem;
  char* Bs = smem + K3_A;
  const int tid = threadIdx.x, wave = tid >> 6, lane = tid & 63;
  const int m0 = (blockIdx.x & 31) * 64, n0 = (blockIdx.x >> 5) * 256;
  stage_kn<512, 8>(win + n0, F, Bs, tid);
  stage_kn<512, 8>(win + (size_t)64 * F + n0, F, Bs + K3_BST, tid);
  {
    const int c = tid & 7, r = tid >> 3;
    u32x4 xr[14];
#pragma unroll
    for (int p = 0; p < 14; p++) xr[p] = *(const u32x4*)(x1T + (size_t)(r + 32 * p) * S + m0 + 8 * c);
    float s1[8], s2[8];
#pragma unroll
    for (int e = 0; e < 8; e++) { s1[e] = 0.f; s2[e] = 0.f; }
#pragma unroll
    for (int p = 0; p < 14; p++) {
      u16 t[8];
      *(u32x4*)t = xr[p];
#pragma unroll
      for (int e = 0; e < 8; e++) { float x = bf2f(t[e]); s1[e] += x; s2[e] += x * x; }
    }
#pragma unroll
    for (int e = 0; e < 8; e++) {
#pragma unroll
      for (int o = 8; o <= 32; o <<= 1) { s1[e] += __shfl_xor(s1[e], o); s2[e] += __shfl_xor(s2[e], o); }
    }
    float* red = (float*)(Bs + 2 * K3_BST);
    if (lane < 8) {
#pragma unroll
      for (int e = 0; e < 8; e++) { red[wave * 128 + (c * 8 + e) * 2] = s1[e]; red[wave * 128 + (c * 8 + e) * 2 + 1] = s2[e]; }
    }
    __syncthreads();
    float mu[8], rs[8];
#pragma unroll
    for (int e = 0; e < 8; e++) {
      const int col = c * 8 + e;
      const float a1 = red[col * 2] + red[128 + col * 2] + red[256 + col * 2] + red[384 + col * 2];
      const float a2 = red[col * 2 + 1] + red[128 + col * 2 + 1] + red[256 + col * 2 + 1] + red[384 + col * 2 + 1];
      const float mean = a1 * (1.f / D);
      const float var = rb(fmaxf(a2 * (1.f / D) - mean * mean, 0.f));
      mu[e] = rb(mean);
      rs[e] = 1.f / rb(sqrtf(rb(var + 1e-5f)));
    }
#pragma unroll
    for (int p = 0; p < 14; p++) {
      const int kr = r + 32 * p;
      const float opc = rb(1.f + modf[4 * D + kr]), sb = modf[3 * D + kr];
      u32x4 o;
#pragma unroll
      for (int e = 0; e < 4; e++) {
        const float h0 = (lo16(xr[p][e]) - mu[2 * e]) * rs[2 * e] * opc + sb;
        const float h1 = (hi16(xr[p][e]) - mu[2 * e + 1]) * rs[2 * e + 1] * opc + sb;
        o[e] = pk2(h0, h1);
      }
      *(u32x4*)(As + kr * 128 + ((c ^ swzB<128>(kr)) << 4)) = o;
    }
  }
  const int wr = wave >> 1, wc = wave & 1;
  f32x4 acc[2][8];
#pragma unroll
  for (int i = 0; i < 2; i++)
#pragma unroll
    for (int j = 0; j < 8; j++) acc[i][j] = (f32x4){0.f, 0.f, 0.f, 0.f};
  for (int kt = 0; kt < 7; kt++) {
    if (kt < 6) { WAIT_V(8); } else { WAIT_V(0); }
    asm volatile("s_waitcnt lgkmcnt(0)" ::: "memory");
    BAR();
    if (kt + 2 < 7) stage_kn<512, 8>(win + (size_t)(kt + 2) * 64 * F + n0, F, Bs + ((kt + 2) % 3) * K3_BST, tid);
    const char* Bt = Bs + (kt % 3) * K3_BST;
#pragma unroll
    for (int ks = 0; ks < 2; ks++) {
      s16x8 af[2], bfr[8];
      TrF ta[2], tb[8];
#pragma unroll
      for (int i = 0; i < 2; i++) tr_issue<128>(ta[i], As, kt * 64 + ks * 32, wr * 32 + i * 16, lane);
#pragma unroll
      for (int j = 0; j < 8; j++) tr_issue<512>(tb[j], Bt, ks * 32, wc * 128 + j * 16, lane);
      TRW2(ta[0], ta[1]); TRW4(tb[0], tb[1], tb[2], tb[3]); TRW4(tb[4], tb[5], tb[6], tb[7]);
#pragma unroll
      for (int i = 0; i < 2; i++) af[i] = tr_get(ta[i]);
#pragma unroll
      for (int j = 0; j < 8; j++) bfr[j] = tr_get(tb[j]);
#pragma unroll
      for (int i = 0; i < 2; i++)
#pragma unroll
        for (int j = 0; j < 8; j++) acc[i][j] = __builtin_amdgcn_mfma_f32_16x16x32_bf16(af[i], bfr[j], acc[i][j], 0, 0, 0);
    }
  }
  __syncthreads();
  u16* Ct = (u16*)smem;
  const int fr = lane & 15, fq = lane >> 4;
#pragma unroll
  for (int i = 0; i < 2; i++) {
#pragma unroll
    for (int reg = 0; reg < 4; reg++) {
      const int row = wr * 32 + i * 16 + fq * 4 + reg;
      float s1 = 0.f, s2 = 0.f;
#pragma unroll
      for (int j = 0; j < 8; j++) {
        const float g = rb(gelu_tanh(rb(acc[i][j][reg])));
        Ct[row * 264 + wc * 128 + j * 16 + fr] = f2bf(g);
        s1 += g; s2 += g * g;
      }
      if (n0 + 256 > FH) {
        const int cbase = n0 + wc * 128;
        if (cbase >= FH) {
#pragma unroll
          for (int o = 1; o < 16; o <<= 1) { s1 += __shfl_xor(s1, o); s2 += __shfl_xor(s2, o); }
          if (fr == 0) { atomicAdd(&zsum[2 * (m0 + row)], s1); atomicAdd(&zsum[2 * (m0 + row) + 1], s2); }
        }
      }
    }
  }
  __syncthreads();
#pragma unroll
  for (int p = 0; p < 8; p++) {
    const int idx = p * 256 + tid;
    const int row = idx >> 5, ch = idx & 31;
    *(u32x4*)(z + (size_t)(m0 + row) * F + n0 + ch * 8) = *(const u32x4*)(Ct + row * 264 + ch * 8);
  }
}

__global__ __launch_bounds__(256) void k3b_norm(const u16* __restrict__ z, const float* __restrict__ zsum,
                                                u16* __restrict__ z2n) {
  const int idx = blockIdx.x * 256 + threadIdx.x;
  const int row = idx / 112, ch = idx % 112;
  u16 t[8];
  *(u32x4*)t = *(const u32x4*)(z + (size_t)row * F + FH + ch * 8);
  const float s1 = zsum[2 * row], s2 = zsum[2 * row + 1];
  const float mean = s1 * (1.f / FH);
  const float var = rb(fmaxf(s2 * (1.f / FH) - mean * mean, 0.f));
  const float mu = rb(mean), sd = 1.f / rb(sqrtf(rb(var + 1e-5f)));
#pragma unroll
  for (int e = 0; e < 8; e++) t[e] = f2bf(rb(bf2f(t[e]) - mu) * sd);
  *(u32x4*)(z2n + (size_t)row * FH + ch * 8) = *(u32x4*)t;
}

constexpr int K4_ST = 128 * 128 + 64 * 128;
__global__ __launch_bounds__(256) void k4_gemm2(const u16* __restrict__ wsp, const u16* __restrict__ z2n,
                                                const u16* __restrict__ z, const u16* __restrict__ bsp,
                                                u16* __restrict__ u) {
  __shared__ __attribute__((aligned(16))) char smem[4 * K4_ST];
  const int tid = threadIdx.x, wave = tid >> 6, lane = tid & 63;
  const int b = blockIdx.x;
  const int xcd = b & 7, idx = b >> 3;
  const int mt = (xcd >> 1) * 4 + (idx & 3), nt = (xcd & 1) * 7 + (idx >> 2);
  const int m0 = mt * 128, n0 = nt * 64;
  const int wr = wave >> 1, wc = wave & 1;
  f32x4 acc[4][2];
#pragma unroll
  for (int i = 0; i < 4; i++)
#pragma unroll
    for (int j = 0; j < 2; j++) acc[i][j] = (f32x4){0.f, 0.f, 0.f, 0.f};
  auto issue = [&](int kt) {
    char* st = smem + (kt & 3) * K4_ST;
    stage_kc<4>(wsp + (size_t)m0 * S + kt * 64, S, st, tid);
    stage_kn<128, 2>(z2n + (size_t)(kt * 64) * FH + n0, FH, st + 128 * 128, tid);
  };
  issue(0); issue(1); issue(2);
  for (int kt = 0; kt < 32; kt++) {
    if (kt < 30) { WAIT_V(12); } else if (kt == 30) { WAIT_V(6); } else { WAIT_V(0); }
    asm volatile("s_waitcnt lgkmcnt(0)" ::: "memory");
    BAR();
    if (kt + 3 < 32) issue(kt + 3);
    const char* At = smem + (kt & 3) * K4_ST;
    const char* Bt = At + 128 * 128;
#pragma unroll
    for (int ks = 0; ks < 2; ks++) {
      s16x8 af[4], bfr[2];
      TrF tb[2];
#pragma unroll
      for (int i = 0; i < 4; i++) {
        const int row = wr * 64 + i * 16 + (lane & 15);
        af[i] = *(const s16x8*)(At + row * 128 + (((ks * 4 + (lane >> 4)) ^ (row & 7)) << 4));
      }
#pragma unroll
      for (int j = 0; j < 2; j++) tr_issue<128>(tb[j], Bt, ks * 32, wc * 32 + j * 16, lane);
      TRW2(tb[0], tb[1]);
      bfr[0] = tr_get(tb[0]); bfr[1] = tr_get(tb[1]);
#pragma unroll
      for (int i = 0; i < 4; i++)
#pragma unroll
        for (int j = 0; j < 2; j++) acc[i][j] = __builtin_amdgcn_mfma_f32_16x16x32_bf16(af[i], bfr[j], acc[i][j], 0, 0, 0);
    }
  }
  __syncthreads();
  float* Ct = (float*)smem;
  const int fr = lane & 15, fq = lane >> 4;
#pragma unroll
  for (int i = 0; i < 4; i++)
#pragma unroll
    for (int reg = 0; reg < 4; reg++) {
      const int row = wr * 64 + i * 16 + fq * 4 + reg;
#pragma unroll
      for (int j = 0; j < 2; j++) Ct[row * 68 + wc * 32 + j * 16 + fr] = acc[i][j][reg];
    }
  __syncthreads();
#pragma unroll
  for (int p = 0; p < 4; p++) {
    const int id = p * 256 + tid;
    const int row = id >> 3, ch = id & 7;
    const float bb = bf2f(bsp[m0 + row]);
    u16 t[8];
    *(u32x4*)t = *(const u32x4*)(z + (size_t)(m0 + row) * F + n0 + ch * 8);
#pragma unroll
    for (int e = 0; e < 8; e++) {
      const float g = rb(rb(Ct[row * 68 + ch * 8 + e]) + bb);
      t[e] = f2bf(bf2f(t[e]) * g);
    }
    *(u32x4*)(u + (size_t)(m0 + row) * FH + n0 + ch * 8) = *(u32x4*)t;
  }
}

constexpr int K5_ST = 64 * 128 + 64 * 128;
__global__ __launch_bounds__(256) void k5_gemm3(const u16* __restrict__ u, const u16* __restrict__ wout,
                                                const u16* __restrict__ x1T, const float* __restrict__ modf,
                                                u16* __restrict__ out) {
  __shared__ __attribute__((aligned(16))) char smem[4 * K5_ST];
  const int tid = threadIdx.x, wave = tid >> 6, lane = tid & 63;
  const int b = blockIdx.x;
  const int xcd = b & 7, idx = b >> 3;
  const int mt = xcd * 4 + (idx & 3), nt = idx >> 2;
  const int m0 = mt * 64, n0 = nt * 64;
  const int wr = wave >> 1, wc = wave & 1;
  f32x4 acc[2][2];
#pragma unroll
  for (int i = 0; i < 2; i++)
#pragma unroll
    for (int j = 0; j < 2; j++) acc[i][j] = (f32x4){0.f, 0.f, 0.f, 0.f};
  auto issue = [&](int kt) {
    char* st = smem + (kt & 3) * K5_ST;
    stage_kc<2>(u + (size_t)m0 * FH + kt * 64, FH, st, tid);
    stage_kn<128, 2>(wout + (size_t)(kt * 64) * D + n0, D, st + 64 * 128, tid);
  };
  issue(0); issue(1); issue(2);
  for (int kt = 0; kt < 14; kt++) {
    if (kt < 12) { WAIT_V(8); } else if (kt == 12) { WAIT_V(4); } else { WAIT_V(0); }
    asm volatile("s_waitcnt lgkmcnt(0)" ::: "memory");
    BAR();
    if (kt + 3 < 14) issue(kt + 3);
    const char* At = smem + (kt & 3) * K5_ST;
    const char* Bt = At + 64 * 128;
#pragma unroll
    for (int ks = 0; ks < 2; ks++) {
      s16x8 af[2], bfr[2];
      TrF tb[2];
#pragma unroll
      for (int i = 0; i < 2; i++) {
        const int row = wr * 32 + i * 16 + (lane & 15);
        af[i] = *(const s16x8*)(At + row * 128 + (((ks * 4 + (lane >> 4)) ^ (row & 7)) << 4));
      }
#pragma unroll
      for (int j = 0; j < 2; j++) tr_issue<128>(tb[j], Bt, ks * 32, wc * 32 + j * 16, lane);
      TRW2(tb[0], tb[1]);
      bfr[0] = tr_get(tb[0]); bfr[1] = tr_get(tb[1]);
#pragma unroll
      for (int i = 0; i < 2; i++)
#pragma unroll
        for (int j = 0; j < 2; j++) acc[i][j] = __builtin_amdgcn_mfma_f32_16x16x32_bf16(af[i], bfr[j], acc[i][j], 0, 0, 0);
    }
  }
  __syncthreads();
  u16* Ct = (u16*)smem;
  const int fr = lane & 15, fq = lane >> 4;
#pragma unroll
  for (int i = 0; i < 2; i++)
#pragma unroll
    for (int j = 0; j < 2; j++) {
      const int col = wc * 32 + j * 16 + fr;
      const int row0 = wr * 32 + i * 16 + fq * 4;
      const float gb = modf[5 * D + n0 + col];
      u16 xv[4];
      *(uint2*)xv = *(const uint2*)(x1T + (size_t)(n0 + col) * S + m0 + row0);
#pragma unroll
      for (int reg = 0; reg < 4; reg++) Ct[(row0 + reg) * 72 + col] = f2bf(bf2f(xv[reg]) + rb(gb * rb(acc[i][j][reg])));
    }
  __syncthreads();
#pragma unroll
  for (int p = 0; p < 2; p++) {
    const int id = p * 256 + tid;
    const int row = id >> 3, ch = id & 7;
    *(u32x4*)(out + (size_t)(m0 + row) * D + n0 + ch * 8) = *(const u32x4*)(Ct + row * 72 + ch * 8);
  }
}

extern "C" void kernel_launch(void* const* d_in, const int* in_sizes, int n_in, void* d_out, int out_size,
                              void* d_ws, size_t ws_size, hipStream_t stream) {
  const u16* q = (const u16*)d_in[0];
  const u16* k = (const u16*)d_in[1];
  const u16* v = (const u16*)d_in[2];
  const u16* shk = (const u16*)d_in[3];
  const u16* ssmk = (const u16*)d_in[4];
  const u16* ssmD = (const u16*)d_in[5];
  const u16* win = (const u16*)d_in[7];
  const u16* wsp = (const u16*)d_in[8];
  const u16* bsp = (const u16*)d_in[9];
  const u16* wout = (const u16*)d_in[10];
  const u16* cvec = (const u16*)d_in[11];
  const u16* wmod = (const u16*)d_in[12];
  u16* out = (u16*)d_out;
  char* ws = (char*)d_ws;
  size_t off = 0;
  auto take = [&](size_t bytes) { char* p = ws + off; off += (bytes + 255) & ~(size_t)255; return p; };
  u16* kvT = (u16*)take((size_t)D * S * 2);
  u16* xT = (u16*)take((size_t)D * S * 2);
  u16* x1T = (u16*)take((size_t)D * S * 2);
  float* xstat = (float*)take((size_t)S * 2 * 4);
  float* modf = (float*)take((size_t)6 * D * 4);
  float* zsum = (float*)take((size_t)S * 2 * 4);
  u16* z = (u16*)take((size_t)S * F * 2);
  u16* z2n = (u16*)take((size_t)S * FH * 2);
  u16* u = (u16*)take((size_t)S * FH * 2);

  k1_prep<<<820, 256, 0, stream>>>(q, k, v, shk, cvec, wmod, kvT, xT, xstat, modf, zsum);
  k2_conv<<<D, 256, 0, stream>>>(kvT, xT, ssmk, ssmD, xstat, modf, x1T);
  k3_gemm1<<<32 * 7, 256, 0, stream>>>(x1T, win, modf, z, zsum);
  k3b_norm<<<896, 256, 0, stream>>>(z, zsum, z2n);
  k4_gemm2<<<16 * 14, 256, 0, stream>>>(wsp, z2n, z, bsp, u);
  k5_gemm3<<<32 * 7, 256, 0, stream>>>(u, wout, x1T, modf, out);
}
```

```cpp
#include <hip/hip_runtime.h>
#include <hip/hip_bf16.h>
#include <stdint.h>

typedef unsigned short u16;
typedef short s16x8 __attribute__((ext_vector_type(8)));
typedef short s16x4 __attribute__((ext_vector_type(4)));
typedef float f32x4 __attribute__((ext_vector_type(4)));
typedef unsigned int u32x4 __attribute__((ext_vector_type(4)));
typedef __attribute__((address_space(3))) s16x4* lds_v4p;

constexpr int S = 2048, D = 448, F = 1792, FH = 896;

__device__ __forceinline__ float bf2f(u16 h) { return __uint_as_float(((unsigned)h) << 16); }
__device__ __forceinline__ u16 f2bf(float f) {
  unsigned u = __float_as_uint(f);
  u += 0x7fffu + ((u >> 16) & 1u);
  return (u16)(u >> 16);
}
typedef __bf16 bf2v __attribute__((ext_vector_type(2)));
typedef float f2v __attribute__((ext_vector_type(2)));
__device__ __forceinline__ unsigned pk2(float a, float b) {
  f2v f = {a, b};
  bf2v r = __builtin_convertvector(f, bf2v);
  return __builtin_bit_cast(unsigned, r);
}
__device__ __forceinline__ float lo16(unsigned u) { return __uint_as_float(u << 16); }
__device__ __forceinline__ float hi16(unsigned u) { return __uint_as_float(u & 0xffff0000u); }
__device__ __forceinline__ float rb(float f) { return lo16(pk2(f, f)); }

#define WAIT_V(n) asm volatile("s_waitcnt vmcnt(" #n ")" ::: "memory")
#define BAR() __builtin_amdgcn_s_barrier()

template <int ROWB>
__device__ __forceinline__ int swzB(int row) {
  if constexpr (ROWB == 128) return (((row >> 1) & 1) << 1) | (((row >> 3) & 1) << 2);
  else return ((row & 1) << 1) | (((row >> 1) & 1) << 2) | (((row >> 3) & 1) << 3);
}

struct TrF { s16x4 lo, hi; };
template <int ROWB>
__device__ __forceinline__ void tr_issue(TrF& f, const char* tile, int kbase, int nbase, int lane) {
  const int g = lane >> 4, q = (lane & 15) >> 2, p = lane & 3;
  const int n = nbase + 4 * p;
  const int r0 = kbase + 8 * g + q, r1 = r0 + 4;
  const unsigned a0 = (unsigned)(size_t)(tile + r0 * ROWB + (((n >> 3) ^ swzB<ROWB>(r0)) << 4) + ((n >> 2) & 1) * 8);
  const unsigned a1 = (unsigned)(size_t)(tile + r1 * ROWB + (((n >> 3) ^ swzB<ROWB>(r1)) << 4) + ((n >> 2) & 1) * 8);
  asm volatile("ds_read_b64_tr_b16 %0, %1" : "=v"(f.lo) : "v"(a0));
  asm volatile("ds_read_b64_tr_b16 %0, %1" : "=v"(f.hi) : "v"(a1));
}
__device__ __forceinline__ s16x8 tr_get(const TrF& f) {
  s16x8 r;
  r[0] = f.lo[0]; r[1] = f.lo[1]; r[2] = f.lo[2]; r[3] = f.lo[3];
  r[4] = f.hi[0]; r[5] = f.hi[1]; r[6] = f.hi[2]; r[7] = f.hi[3];
  return r;
}
#define TRW2(a, b) asm volatile("s_waitcnt lgkmcnt(0)" : "+v"(a.lo), "+v"(a.hi), "+v"(b.lo), "+v"(b.hi))
#define TRW4(a, b, c, d) asm volatile("s_waitcnt lgkmcnt(0)" : "+v"(a.lo), "+v"(a.hi), "+v"(b.lo), "+v"(b.hi), "+v"(c.lo), "+v"(c.hi), "+v"(d.lo), "+v"(d.hi))

__global__ __launch_bounds__(256) void k1_prep(const u16* __restrict__ q, const u16* __restrict__ k,
                                               const u16* __restrict__ v, const u16* __restrict__ shk,
                                               const u16* __restrict__ cvec, const u16* __restrict__ wmod,
                                               u16* __restrict__ kvT, u16* __restrict__ xT,
                                               float* __restrict__ xstat, float* __restrict__ modf,
                                               float* __restrict__ zsum) {
  __shared__ __attribute__((aligned(16))) char smem[2 * 64 * 72 * 2];
  const int b = blockIdx.x, tid = threadIdx.x;
  if (b < 224) {
    u16* Tkv = (u16*)smem;
    u16* Tx = Tkv + 64 * 72;
    const int st = b / 7, dt = b % 7;
    const int s0 = st * 64, d0 = dt * 64;
    const int c = tid & 7, r = tid >> 3;
    u16 sh[32];
    u16 qq[2][8], vv[2][8], kk[2][4][8];
    {
      const u32x4* sp = (const u32x4*)(shk + (size_t)(d0 + 8 * c) * 4);
#pragma unroll
      for (int i = 0; i < 4; i++) *(u32x4*)&sh[8 * i] = sp[i];
    }
#pragma unroll
    for (int pass = 0; pass < 2; pass++) {
      const int s = s0 + r + 32 * pass;
      *(u32x4*)qq[pass] = *(const u32x4*)(q + (size_t)s * D + d0 + 8 * c);
      *(u32x4*)vv[pass] = *(const u32x4*)(v + (size_t)s * D + d0 + 8 * c);
#pragma unroll
      for (int j = 0; j < 4; j++) {
        const int sj = s - j >= 0 ? s - j : 0;
        u32x4 t = *(const u32x4*)(k + (size_t)sj * D + d0 + 8 * c);
        if (s - j < 0) t = (u32x4){0, 0, 0, 0};
        *(u32x4*)kk[pass][j] = t;
      }
    }
#pragma unroll
    for (int pass = 0; pass < 2; pass++) {
      const int row = r + 32 * pass;
#pragma unroll
      for (int e = 0; e < 8; e++) {
        float acc = 0.f;
#pragma unroll
        for (int j = 3; j >= 0; j--) acc = rb(acc + rb(bf2f(kk[pass][j][e]) * bf2f(sh[e * 4 + j])));
        Tkv[(8 * c + e) * 72 + row] = f2bf(acc * bf2f(vv[pass][e]));
        Tx[(8 * c + e) * 72 + row] = qq[pass][e];
      }
    }
    __syncthreads();
#pragma unroll
    for (int pass = 0; pass < 2; pass++) {
      const int dr = r + 32 * pass;
      *(u32x4*)(kvT + (size_t)(d0 + dr) * S + s0 + 8 * c) = *(const u32x4*)(Tkv + dr * 72 + 8 * c);
      *(u32x4*)(xT + (size_t)(d0 + dr) * S + s0 + 8 * c) = *(const u32x4*)(Tx + dr * 72 + 8 * c);
    }
  } else if (b < 736) {
    const int wave = tid >> 6, lane = tid & 63;
    const int s = (b - 224) * 4 + wave;
    float xv[8];
    float sum = 0.f;
    if (lane < 56) {
      u16 t[8];
      *(u32x4*)t = *(const u32x4*)(q + (size_t)s * D + lane * 8);
#pragma unroll
      for (int e = 0; e < 8; e++) { xv[e] = bf2f(t[e]); sum += xv[e]; }
    } else {
#pragma unroll
      for (int e = 0; e < 8; e++) xv[e] = 0.f;
    }
#pragma unroll
    for (int o = 32; o >= 1; o >>= 1) sum += __shfl_xor(sum, o);
    const float mean = sum * (1.f / D);
    float sq = 0.f;
    if (lane < 56) {
#pragma unroll
      for (int e = 0; e < 8; e++) { float dd = xv[e] - mean; sq += dd * dd; }
    }
#pragma unroll
    for (int o = 32; o >= 1; o >>= 1) sq += __shfl_xor(sq, o);
    if (lane == 0) {
      const float var = rb(sq * (1.f / D));
      xstat[2 * s] = rb(mean);
      xstat[2 * s + 1] = 1.f / rb(sqrtf(rb(var + 1e-5f)));
    }
    if (lane == 2 || lane == 3) zsum[2 * s + lane - 2] = 0.f;
  } else {
    float* red = (float*)smem;
    const int n0 = (b - 736) * 32;
    const int c4 = tid & 3, ks = tid >> 2;
    u32x4 t[7];
    float cv[7];
#pragma unroll
    for (int kk = 0; kk < 7; kk++) {
      const int kr = ks * 7 + kk;
      t[kk] = *(const u32x4*)(wmod + (size_t)kr * (6 * D) + n0 + 8 * c4);
      cv[kk] = bf2f(cvec[kr]);
    }
    float acc[8];
#pragma unroll
    for (int e = 0; e < 8; e++) acc[e] = 0.f;
#pragma unroll
    for (int kk = 0; kk < 7; kk++) {
      const float sc = rb(cv[kk] / (1.f + __expf(-cv[kk])));
      u16 tt[8];
      *(u32x4*)tt = t[kk];
#pragma unroll
      for (int e = 0; e < 8; e++) acc[e] += sc * bf2f(tt[e]);
    }
#pragma unroll
    for (int e = 0; e < 8; e++) red[ks * 32 + c4 * 8 + e] = acc[e];
    __syncthreads();
    if (tid < 32) {
      float sum = 0.f;
#pragma unroll
      for (int i = 0; i < 64; i++) sum += red[i * 32 + tid];
      modf[n0 + tid] = rb(sum);
    }
  }
}

#define RSK(i) ((i) + (((i) >> 7) << 3))
__global__ __launch_bounds__(256) void k2_conv(const u16* __restrict__ kvT, const u16* __restrict__ xT,
                                               const u16* __restrict__ ssmk, const u16* __restrict__ ssmD,
                                               const float* __restrict__ xstat, const float* __restrict__ modf,
                                               u16* __restrict__ x1T) {
  __shared__ __attribute__((aligned(16))) u16 KFs[2208];
  __shared__ __attribute__((aligned(16))) u16 Rs[4112 + 8 * 33];
  __shared__ __attribute__((aligned(16))) float ybuf[4][2112];
  const int d = blockIdx.x, tid = threadIdx.x;
  const int wave = tid >> 6, lane = tid & 63;
  u16 kvv[8], xv[8];
  f32x4 st[4];
  const u32x4 kf = *(const u32x4*)(ssmk + (size_t)d * S + 8 * tid);
  *(u32x4*)kvv = *(const u32x4*)(kvT + (size_t)d * S + 8 * tid);
  *(u32x4*)xv = *(const u32x4*)(xT + (size_t)d * S + 8 * tid);
#pragma unroll
  for (int i = 0; i < 4; i++) st[i] = *(const f32x4*)(xstat + 16 * tid + 4 * i);
  const float Dd = bf2f(ssmD[d]);
  const float sa = modf[d], opca = rb(1.f + modf[D + d]), ga = modf[2 * D + d];
  {
    u32x4 z = {0, 0, 0, 0};
    if (tid < 16) *(u32x4*)&KFs[8 * tid] = z;
    if (tid < 4) *(u32x4*)&KFs[2176 + 8 * tid] = z;
    *(u32x4*)&KFs[128 + 8 * tid] = kf;
    u16 tr[8];
#pragma unroll
    for (int e = 0; e < 8; e++) tr[7 - e] = kvv[e];
    *(u32x4*)&Rs[RSK(2040 - 8 * tid + 8)] = *(u32x4*)tr;
    if (tid == 0) *(u32x4*)&Rs[0] = z;
    *(u32x4*)&Rs[RSK(2056 + 8 * tid)] = z;
    if (tid == 0) *(u32x4*)&Rs[RSK(2056 + 8 * 256)] = z;
  }
  __syncthreads();
  const int m = lane & 15, h = lane >> 4;
  f32x4 acc[8];
#pragma unroll
  for (int p = 0; p < 8; p++) acc[p] = (f32x4){0.f, 0.f, 0.f, 0.f};
  const int tbeg = wave == 0 ? 0 : 17 + 16 * (wave - 1);
  const int tend = wave == 0 ? 17 : tbeg + 16;
#pragma unroll 2
  for (int t = tbeg; t < tend; t++) {
    const int q0 = -128 + 32 * t;
    const s16x8 a = *(const s16x8*)&KFs[8 * m + q0 + 8 * h + 128];
    const int basep = 2040 - 128 * m + q0 + 8 * h;
    const u32x4 w0 = *(const u32x4*)&Rs[RSK(basep + 8)];
    const u32x4 w1 = *(const u32x4*)&Rs[RSK(basep + 16)];
    unsigned W[8] = {w0[0], w0[1], w0[2], w0[3], w1[0], w1[1], w1[2], w1[3]};
#pragma unroll
    for (int p = 0; p < 8; p++) {
      const int sft = 7 - p;
      u32x4 fr;
      if ((sft & 1) == 0) {
#pragma unroll
        for (int i = 0; i < 4; i++) fr[i] = W[sft / 2 + i];
      } else {
#pragma unroll
        for (int i = 0; i < 4; i++) fr[i] = __builtin_amdgcn_alignbit(W[(sft - 1) / 2 + i + 1], W[(sft - 1) / 2 + i], 16);
      }
      acc[p] = __builtin_amdgcn_mfma_f32_16x16x32_bf16(a, *(s16x8*)&fr, acc[p], 0, 0, 0);
    }
  }
  {
    const int i = lane & 15;
#pragma unroll
    for (int reg = 0; reg < 4; reg++) {
      const int mm = 4 * h + reg;
      const int s = 128 * i + 8 * mm;
      f32x4 v0 = {acc[0][reg], acc[1][reg], acc[2][reg], acc[3][reg]};
      f32x4 v1 = {acc[4][reg], acc[5][reg], acc[6][reg], acc[7][reg]};
      *(f32x4*)&ybuf[wave][s + 4 * i] = v0;
      *(f32x4*)&ybuf[wave][s + 4 * i + 4] = v1;
    }
  }
  __syncthreads();
  {
    const int s0 = 8 * tid;
    const int idx = s0 + 4 * (s0 >> 7);
    float y[8];
#pragma unroll
    for (int e = 0; e < 8; e++) y[e] = 0.f;
#pragma unroll
    for (int w = 0; w < 4; w++) {
      f32x4 v0 = *(const f32x4*)&ybuf[w][idx];
      f32x4 v1 = *(const f32x4*)&ybuf[w][idx + 4];
#pragma unroll
      for (int e = 0; e < 4; e++) { y[e] += v0[e]; y[4 + e] += v1[e]; }
    }
    u16 o[8];
#pragma unroll
    for (int e = 0; e < 8; e++) {
      const float mean = st[e >> 1][(e & 1) * 2], sd = st[e >> 1][(e & 1) * 2 + 1];
      const float yy = rb(y[e]);
      const float y2 = rb(yy + rb(bf2f(kvv[e]) * Dd));
      const float x = bf2f(xv[e]);
      const float ln = rb(rb(x - mean) * sd);
      const float hh = rb(rb(ln * opca) + sa);
      const float a = rb(hh * y2);
      o[e] = f2bf(x + rb(ga * a));
    }
    *(u32x4*)(x1T + (size_t)d * S + s0) = *(u32x4*)o;
  }
}

template <int NI>
__device__ __forceinline__ void stage_kc(const u16* __restrict__ g, int ld, char* lds, int tid) {
#pragma unroll
  for (int i = 0; i < NI; i++) {
    const int b = i * 4096 + tid * 16;
    const int row = b >> 7, ch = (b >> 4) & 7;
    const int sch = ch ^ (row & 7);
    __builtin_amdgcn_global_load_lds((const unsigned*)(g + (size_t)row * ld + sch * 8), (unsigned*)(lds + b), 16, 0, 0);
  }
}
template <int ROWB, int NI>
__device__ __forceinline__ void stage_kn(const u16* __restrict__ g, int ld, char* lds, int tid) {
#pragma unroll
  for (int i = 0; i < NI; i++) {
    const int b = i * 4096 + tid * 16;
    const int row = b / ROWB, ch = (b % ROWB) >> 4;
    const int sch = ch ^ swzB<ROWB>(row);
    __builtin_amdgcn_global_load_lds((const unsigned*)(g + (size_t)row * ld + sch * 8), (unsigned*)(lds + b), 16, 0, 0);
  }
}

__device__ __forceinline__ float gelu_tanh(float x) {
  const float y = x * (2.302208198f + 0.1029432397f * x * x);
  const float e = __builtin_amdgcn_exp2f(-y);
  return x * __builtin_amdgcn_rcpf(1.f + e);
}

constexpr int K3_A = 448 * 128;
constexpr int K3_BST = 64 * 512;
__global__ __launch_bounds__(256) void k3_gemm1(const u16* __restrict__ x1T, const u16* __restrict__ win,
                                                const float* __restrict__ modf, u16* __restrict__ z,
                                                float* __restrict__ zsum) {
  __shared__ __attribute__((aligned(16))) char smem[K3_A + 3 * K3_BST];
  char* As = smem;
  char* Bs = smem + K3_A;
  const int tid = threadIdx.x, wave = tid >> 6, lane = tid & 63;
  const int m0 = (blockIdx.x & 31) * 64, n0 = (blockIdx.x >> 5) * 256;
  stage_kn<512, 8>(win + n0, F, Bs, tid);
  stage_kn<512, 8>(win + (size_t)64 * F + n0, F, Bs + K3_BST, tid);
  {
    const int c = tid & 7, r = tid >> 3;
    u32x4 xr[14];
#pragma unroll
    for (int p = 0; p < 14; p++) xr[p] = *(const u32x4*)(x1T + (size_t)(r + 32 * p) * S + m0 + 8 * c);
    float s1[8], s2[8];
#pragma unroll
    for (int e = 0; e < 8; e++) { s1[e] = 0.f; s2[e] = 0.f; }
#pragma unroll
    for (int p = 0; p < 14; p++) {
      u16 t[8];
      *(u32x4*)t = xr[p];
#pragma unroll
      for (int e = 0; e < 8; e++) { float x = bf2f(t[e]); s1[e] += x; s2[e] += x * x; }
    }
#pragma unroll
    for (int e = 0; e < 8; e++) {
#pragma unroll
      for (int o = 8; o <= 32; o <<= 1) { s1[e] += __shfl_xor(s1[e], o); s2[e] += __shfl_xor(s2[e], o); }
    }
    float* red = (float*)(Bs + 2 * K3_BST);
    if (lane < 8) {
#pragma unroll
      for (int e = 0; e < 8; e++) { red[wave * 128 + (c * 8 + e) * 2] = s1[e]; red[wave * 128 + (c * 8 + e) * 2 + 1] = s2[e]; }
    }
    __syncthreads();
    float mu[8], rs[8];
#pragma unroll
    for (int e = 0; e < 8; e++) {
      const int col = c * 8 + e;
      const float a1 = red[col * 2] + red[128 + col * 2] + red[256 + col * 2] + red[384 + col * 2];
      const float a2 = red[col * 2 + 1] + red[128 + col * 2 + 1] + red[256 + col * 2 + 1] + red[384 + col * 2 + 1];
      const float mean = a1 * (1.f / D);
      const float var = rb(fmaxf(a2 * (1.f / D) - mean * mean, 0.f));
      mu[e] = rb(mean);
      rs[e] = 1.f / rb(sqrtf(rb(var + 1e-5f)));
    }
#pragma unroll
    for (int p = 0; p < 14; p++) {
      const int kr = r + 32 * p;
      const float opc = rb(1.f + modf[4 * D + kr]), sb = modf[3 * D + kr];
      u32x4 o;
#pragma unroll
      for (int e = 0; e < 4; e++) {
        const float h0 = (lo16(xr[p][e]) - mu[2 * e]) * rs[2 * e] * opc + sb;
        const float h1 = (hi16(xr[p][e]) - mu[2 * e + 1]) * rs[2 * e + 1] * opc + sb;
        o[e] = pk2(h0, h1);
      }
      *(u32x4*)(As + kr * 128 + ((c ^ swzB<128>(kr)) << 4)) = o;
    }
  }
  const int wr = wave >> 1, wc = wave & 1;
  f32x4 acc[2][8];
#pragma unroll
  for (int i = 0; i < 2; i++)
#pragma unroll
    for (int j = 0; j < 8; j++) acc[i][j] = (f32x4){0.f, 0.f, 0.f, 0.f};
  for (int kt = 0; kt < 7; kt++) {
    if (kt < 6) { WAIT_V(8); } else { WAIT_V(0); }
    asm volatile("s_waitcnt lgkmcnt(0)" ::: "memory");
    BAR();
    if (kt + 2 < 7) stage_kn<512, 8>(win + (size_t)(kt + 2) * 64 * F + n0, F, Bs + ((kt + 2) % 3) * K3_BST, tid);
    const char* Bt = Bs + (kt % 3) * K3_BST;
#pragma unroll
    for (int ks = 0; ks < 2; ks++) {
      s16x8 af[2], bfr[8];
      TrF ta[2], tb[8];
#pragma unroll
      for (int i = 0; i < 2; i++) tr_issue<128>(ta[i], As, kt * 64 + ks * 32, wr * 32 + i * 16, lane);
#pragma unroll
      for (int j = 0; j < 8; j++) tr_issue<512>(tb[j], Bt, ks * 32, wc * 128 + j * 16, lane);
      TRW2(ta[0], ta[1]); TRW4(tb[0], tb[1], tb[2], tb[3]); TRW4(tb[4], tb[5], tb[6], tb[7]);
#pragma unroll
      for (int i = 0; i < 2; i++) af[i] = tr_get(ta[i]);
#pragma unroll
      for (int j = 0; j < 8; j++) bfr[j] = tr_get(tb[j]);
#pragma unroll
      for (int i = 0; i < 2; i++)
#pragma unroll
        for (int j = 0; j < 8; j++) acc[i][j] = __builtin_amdgcn_mfma_f32_16x16x32_bf16(af[i], bfr[j], acc[i][j], 0, 0, 0);
    }
  }
  __syncthreads();
  u16* Ct = (u16*)smem;
  const int fr = lane & 15, fq = lane >> 4;
#pragma unroll
  for (int i = 0; i < 2; i++) {
#pragma unroll
    for (int reg = 0; reg < 4; reg++) {
      const int row = wr * 32 + i * 16 + fq * 4 + reg;
      float s1 = 0.f, s2 = 0.f;
#pragma unroll
      for (int j = 0; j < 8; j++) {
        const float g = rb(gelu_tanh(rb(acc[i][j][reg])));
        Ct[row * 264 + wc * 128 + j * 16 + fr] = f2bf(g);
        s1 += g; s2 += g * g;
      }
      if (n0 + 256 > FH) {
        const int cbase = n0 + wc * 128;
        if (cbase >= FH) {
#pragma unroll
          for (int o = 1; o < 16; o <<= 1) { s1 += __shfl_xor(s1, o); s2 += __shfl_xor(s2, o); }
          if (fr == 0) { atomicAdd(&zsum[2 * (m0 + row)], s1); atomicAdd(&zsum[2 * (m0 + row) + 1], s2); }
        }
      }
    }
  }
  __syncthreads();
#pragma unroll
  for (int p = 0; p < 8; p++) {
    const int idx = p * 256 + tid;
    const int row = idx >> 5, ch = idx & 31;
    *(u32x4*)(z + (size_t)(m0 + row) * F + n0 + ch * 8) = *(const u32x4*)(Ct + row * 264 + ch * 8);
  }
}

__global__ __launch_bounds__(256) void k3b_norm(const u16* __restrict__ z, const float* __restrict__ zsum,
                                                u16* __restrict__ z2n) {
  const int idx = blockIdx.x * 256 + threadIdx.x;
  const int row = idx / 112, ch = idx % 112;
  u16 t[8];
  *(u32x4*)t = *(const u32x4*)(z + (size_t)row * F + FH + ch * 8);
  const float s1 = zsum[2 * row], s2 = zsum[2 * row + 1];
  const float mean = s1 * (1.f / FH);
  const float var = rb(fmaxf(s2 * (1.f / FH) - mean * mean, 0.f));
  const float mu = rb(mean), sd = 1.f / rb(sqrtf(rb(var + 1e-5f)));
#pragma unroll
  for (int e = 0; e < 8; e++) t[e] = f2bf(rb(bf2f(t[e]) - mu) * sd);
  *(u32x4*)(z2n + (size_t)row * FH + ch * 8) = *(u32x4*)t;
}

constexpr int K4_ST = 128 * 128 + 64 * 128;
template <int NI>
__device__ __forceinline__ void stage_kc512(const u16* __restrict__ g, int ld, char* lds, int tid) {
#pragma unroll
  for (int i = 0; i < NI; i++) {
    const int b = i * 8192 + tid * 16;
    const int row = b >> 7, ch = (b >> 4) & 7;
    const int sch = ch ^ (row & 7);
    __builtin_amdgcn_global_load_lds((const unsigned*)(g + (size_t)row * ld + sch * 8), (unsigned*)(lds + b), 16, 0, 0);
  }
}
template <int ROWB, int NI>
__device__ __forceinline__ void stage_kn512(const u16* __restrict__ g, int ld, char* lds, int tid) {
#pragma unroll
  for (int i = 0; i < NI; i++) {
    const int b = i * 8192 + tid * 16;
    const int row = b / ROWB, ch = (b % ROWB) >> 4;
    const int sch = ch ^ swzB<ROWB>(row);
    __builtin_amdgcn_global_load_lds((const unsigned*)(g + (size_t)row * ld + sch * 8), (unsigned*)(lds + b), 16, 0, 0);
  }
}
__global__ __launch_bounds__(512) void k4_gemm2(const u16* __restrict__ wsp, const u16* __restrict__ z2n,
                                                const u16* __restrict__ z, const u16* __restrict__ bsp,
                                                u16* __restrict__ u) {
  __shared__ __attribute__((aligned(16))) char smem[4 * K4_ST];
  const int tid = threadIdx.x, wave = tid >> 6, lane = tid & 63;
  const int b = blockIdx.x;
  const int xcd = b & 7, idx = b >> 3;
  const int mt = (xcd >> 1) * 4 + (idx & 3), nt = (xcd & 1) * 7 + (idx >> 2);
  const int m0 = mt * 128, n0 = nt * 64;
  const int koff = 0;
  const int wr = wave >> 1, wc = wave & 1;
  f32x4 acc[2][2];
#pragma unroll
  for (int i = 0; i < 2; i++)
#pragma unroll
    for (int j = 0; j < 2; j++) acc[i][j] = (f32x4){0.f, 0.f, 0.f, 0.f};
  auto issue = [&](int kt) {
    char* st = smem + (kt & 3) * K4_ST;
    const int ke = (kt + koff) & 31;
    stage_kc512<2>(wsp + (size_t)m0 * S + ke * 64, S, st, tid);
    stage_kn512<128, 1>(z2n + (size_t)(ke * 64) * FH + n0, FH, st + 128 * 128, tid);
  };
  issue(0); issue(1); issue(2);
  for (int kt = 0; kt < 32; kt++) {
    if (kt < 30) { WAIT_V(6); } else if (kt == 30) { WAIT_V(3); } else { WAIT_V(0); }
    asm volatile("s_waitcnt lgkmcnt(0)" ::: "memory");
    BAR();
    if (kt + 3 < 32) issue(kt + 3);
    const char* At = smem + (kt & 3) * K4_ST;
    const char* Bt = At + 128 * 128;
#pragma unroll
    for (int ks = 0; ks < 2; ks++) {
      s16x8 af[2], bfr[2];
      TrF tb[2];
#pragma unroll
      for (int i = 0; i < 2; i++) {
        const int row = wr * 32 + i * 16 + (lane & 15);
        af[i] = *(const s16x8*)(At + row * 128 + (((ks * 4 + (lane >> 4)) ^ (row & 7)) << 4));
      }
#pragma unroll
      for (int j = 0; j < 2; j++) tr_issue<128>(tb[j], Bt, ks * 32, wc * 32 + j * 16, lane);
      TRW2(tb[0], tb[1]);
      bfr[0] = tr_get(tb[0]); bfr[1] = tr_get(tb[1]);
#pragma unroll
      for (int i = 0; i < 2; i++)
#pragma unroll
        for (int j = 0; j < 2; j++) acc[i][j] = __builtin_amdgcn_mfma_f32_16x16x32_bf16(af[i], bfr[j], acc[i][j], 0, 0, 0);
    }
  }
  __syncthreads();
  float* Ct = (float*)smem;
  const int fr = lane & 15, fq = lane >> 4;
#pragma unroll
  for (int i = 0; i < 2; i++)
#pragma unroll
    for (int reg = 0; reg < 4; reg++) {
      const int row = wr * 32 + i * 16 + fq * 4 + reg;
#pragma unroll
      for (int j = 0; j < 2; j++) Ct[row * 68 + wc * 32 + j * 16 + fr] = acc[i][j][reg];
    }
  __syncthreads();
#pragma unroll
  for (int p = 0; p < 2; p++) {
    const int id = p * 512 + tid;
    const int row = id >> 3, ch = id & 7;
    const float bb = bf2f(bsp[m0 + row]);
    u16 t[8];
    *(u32x4*)t = *(const u32x4*)(z + (size_t)(m0 + row) * F + n0 + ch * 8);
#pragma unroll
    for (int e = 0; e < 8; e++) {
      const float g = rb(rb(Ct[row * 68 + ch * 8 + e]) + bb);
      t[e] = f2bf(bf2f(t[e]) * g);
    }
    *(u32x4*)(u + (size_t)(m0 + row) * FH + n0 + ch * 8) = *(u32x4*)t;
  }
}

constexpr int K5_ST = 64 * 128 + 64 * 128;
__global__ __launch_bounds__(256) void k5_gemm3(const u16* __restrict__ u, const u16* __restrict__ wout,
                                                const u16* __restrict__ x1T, const float* __restrict__ modf,
                                                u16* __restrict__ out) {
  __shared__ __attribute__((aligned(16))) char smem[4 * K5_ST];
  const int tid = threadIdx.x, wave = tid >> 6, lane = tid & 63;
  const int b = blockIdx.x;
  const int xcd = b & 7, idx = b >> 3;
  const int mt = xcd * 4 + (idx & 3), nt = idx >> 2;
  const int m0 = mt * 64, n0 = nt * 64;
  const int wr = wave >> 1, wc = wave & 1;
  f32x4 acc[2][2];
#pragma unroll
  for (int i = 0; i < 2; i++)
#pragma unroll
    for (int j = 0; j < 2; j++) acc[i][j] = (f32x4){0.f, 0.f, 0.f, 0.f};
  auto issue = [&](int kt) {
    char* st = smem + (kt & 3) * K5_ST;
    stage_kc<2>(u + (size_t)m0 * FH + kt * 64, FH, st, tid);
    stage_kn<128, 2>(wout + (size_t)(kt * 64) * D + n0, D, st + 64 * 128, tid);
  };
  issue(0); issue(1); issue(2);
  for (int kt = 0; kt < 14; kt++) {
    if (kt < 12) { WAIT_V(8); } else if (kt == 12) { WAIT_V(4); } else { WAIT_V(0); }
    asm volatile("s_waitcnt lgkmcnt(0)" ::: "memory");
    BAR();
    if (kt + 3 < 14) issue(kt + 3);
    const char* At = smem + (kt & 3) * K5_ST;
    const char* Bt = At + 64 * 128;
#pragma unroll
    for (int ks = 0; ks < 2; ks++) {
      s16x8 af[2], bfr[2];
      TrF tb[2];
#pragma unroll
      for (int i = 0; i < 2; i++) {
        const int row = wr * 32 + i * 16 + (lane & 15);
        af[i] = *(const s16x8*)(At + row * 128 + (((ks * 4 + (lane >> 4)) ^ (row & 7)) << 4));
      }
#pragma unroll
      for (int j = 0; j < 2; j++) tr_issue<128>(tb[j], Bt, ks * 32, wc * 32 + j * 16, lane);
      TRW2(tb[0], tb[1]);
      bfr[0] = tr_get(tb[0]); bfr[1] = tr_get(tb[1]);
#pragma unroll
      for (int i = 0; i < 2; i++)
#pragma unroll
        for (int j = 0; j < 2; j++) acc[i][j] = __builtin_amdgcn_mfma_f32_16x16x32_bf16(af[i], bfr[j], acc[i][j], 0, 0, 0);
    }
  }
  __syncthreads();
  u16* Ct = (u16*)smem;
  const int fr = lane & 15, fq = lane >> 4;
#pragma unroll
  for (int i = 0; i < 2; i++)
#pragma unroll
    for (int j = 0; j < 2; j++) {
      const int col = wc * 32 + j * 16 + fr;
      const int row0 = wr * 32 + i * 16 + fq * 4;
      const float gb = modf[5 * D + n0 + col];
      u16 xv[4];
      *(uint2*)xv = *(const uint2*)(x1T + (size_t)(n0 + col) * S + m0 + row0);
#pragma unroll
      for (int reg = 0; reg < 4; reg++) Ct[(row0 + reg) * 72 + col] = f2bf(bf2f(xv[reg]) + rb(gb * rb(acc[i][j][reg])));
    }
  __syncthreads();
#pragma unroll
  for (int p = 0; p < 2; p++) {
    const int id = p * 256 + tid;
    const int row = id >> 3, ch = id & 7;
    *(u32x4*)(out + (size_t)(m0 + row) * D + n0 + ch * 8) = *(const u32x4*)(Ct + row * 72 + ch * 8);
  }
}

extern "C" void kernel_launch(void* const* d_in, const int* in_sizes, int n_in, void* d_out, int out_size,
                              void* d_ws, size_t ws_size, hipStream_t stream) {
  const u16* q = (const u16*)d_in[0];
  const u16* k = (const u16*)d_in[1];
  const u16* v = (const u16*)d_in[2];
  const u16* shk = (const u16*)d_in[3];
  const u16* ssmk = (const u16*)d_in[4];
  const u16* ssmD = (const u16*)d_in[5];
  const u16* win = (const u16*)d_in[7];
  const u16* wsp = (const u16*)d_in[8];
  const u16* bsp = (const u16*)d_in[9];
  const u16* wout = (const u16*)d_in[10];
  const u16* cvec = (const u16*)d_in[11];
  const u16* wmod = (const u16*)d_in[12];
  u16* out = (u16*)d_out;
  char* ws = (char*)d_ws;
  size_t off = 0;
  auto take = [&](size_t bytes) { char* p = ws + off; off += (bytes + 255) & ~(size_t)255; return p; };
  u16* kvT = (u16*)take((size_t)D * S * 2);
  u16* xT = (u16*)take((size_t)D * S * 2);
  u16* x1T = (u16*)take((size_t)D * S * 2);
  float* xstat = (float*)take((size_t)S * 2 * 4);
  float* modf = (float*)take((size_t)6 * D * 4);
  float* zsum = (float*)take((size_t)S * 2 * 4);
  u16* z = (u16*)take((size_t)S * F * 2);
  u16* z2n = (u16*)take((size_t)S * FH * 2);
  u16* u = (u16*)take((size_t)S * FH * 2);

  k1_prep<<<820, 256, 0, stream>>>(q, k, v, shk, cvec, wmod, kvT, xT, xstat, modf, zsum);
  k2_conv<<<D, 256, 0, stream>>>(kvT, xT, ssmk, ssmD, xstat, modf, x1T);
  k3_gemm1<<<32 * 7, 256, 0, stream>>>(x1T, win, modf, z, zsum);
  k3b_norm<<<896, 256, 0, stream>>>(z, zsum, z2n);
  k4_gemm2<<<16 * 14, 512, 0, stream>>>(wsp, z2n, z, bsp, u);
  k5_gemm3<<<32 * 7, 256, 0, stream>>>(u, wout, x1T, modf, out);
}
```

```cpp
#include <hip/hip_runtime.h>
#include <hip/hip_bf16.h>
#include <stdint.h>

typedef unsigned short u16;
typedef short s16x8 __attribute__((ext_vector_type(8)));
typedef short s16x4 __attribute__((ext_vector_type(4)));
typedef float f32x4 __attribute__((ext_vector_type(4)));
typedef unsigned int u32x4 __attribute__((ext_vector_type(4)));
typedef __attribute__((address_space(3))) s16x4* lds_v4p;

constexpr int S = 2048, D = 448, F = 1792, FH = 896;

__device__ __forceinline__ float bf2f(u16 h) { return __uint_as_float(((unsigned)h) << 16); }
__device__ __forceinline__ u16 f2bf(float f) {
  unsigned u = __float_as_uint(f);
  u += 0x7fffu + ((u >> 16) & 1u);
  return (u16)(u >> 16);
}
typedef __bf16 bf2v __attribute__((ext_vector_type(2)));
typedef float f2v __attribute__((ext_vector_type(2)));
__device__ __forceinline__ unsigned pk2(float a, float b) {
  f2v f = {a, b};
  bf2v r = __builtin_convertvector(f, bf2v);
  return __builtin_bit_cast(unsigned, r);
}
__device__ __forceinline__ float lo16(unsigned u) { return __uint_as_float(u << 16); }
__device__ __forceinline__ float hi16(unsigned u) { return __uint_as_float(u & 0xffff0000u); }
__device__ __forceinline__ float rb(float f) { return lo16(pk2(f, f)); }

#define WAIT_V(n) asm volatile("s_waitcnt vmcnt(" #n ")" ::: "memory")
#define BAR() __builtin_amdgcn_s_barrier()

template <int ROWB>
__device__ __forceinline__ int swzB(int row) {
  if constexpr (ROWB == 128) return (((row >> 1) & 1) << 1) | (((row >> 3) & 1) << 2);
  else return ((row & 1) << 1) | (((row >> 1) & 1) << 2) | (((row >> 3) & 1) << 3);
}

struct TrF { s16x4 lo, hi; };
template <int ROWB>
__device__ __forceinline__ void tr_issue(TrF& f, const char* tile, int kbase, int nbase, int lane) {
  const int g = lane >> 4, q = (lane & 15) >> 2, p = lane & 3;
  const int n = nbase + 4 * p;
  const int r0 = kbase + 8 * g + q, r1 = r0 + 4;
  const unsigned a0 = (unsigned)(size_t)(tile + r0 * ROWB + (((n >> 3) ^ swzB<ROWB>(r0)) << 4) + ((n >> 2) & 1) * 8);
  const unsigned a1 = (unsigned)(size_t)(tile + r1 * ROWB + (((n >> 3) ^ swzB<ROWB>(r1)) << 4) + ((n >> 2) & 1) * 8);
  asm volatile("ds_read_b64_tr_b16 %0, %1" : "=v"(f.lo) : "v"(a0));
  asm volatile("ds_read_b64_tr_b16 %0, %1" : "=v"(f.hi) : "v"(a1));
}
__device__ __forceinline__ s16x8 tr_get(const TrF& f) {
  s16x8 r;
  r[0] = f.lo[0]; r[1] = f.lo[1]; r[2] = f.lo[2]; r[3] = f.lo[3];
  r[4] = f.hi[0]; r[5] = f.hi[1]; r[6] = f.hi[2]; r[7] = f.hi[3];
  return r;
}
#define TRW2(a, b) asm volatile("s_waitcnt lgkmcnt(0)" : "+v"(a.lo), "+v"(a.hi), "+v"(b.lo), "+v"(b.hi))
#define TRW4(a, b, c, d) asm volatile("s_waitcnt lgkmcnt(0)" : "+v"(a.lo), "+v"(a.hi), "+v"(b.lo), "+v"(b.hi), "+v"(c.lo), "+v"(c.hi), "+v"(d.lo), "+v"(d.hi))

__global__ __launch_bounds__(256) void k1_prep(const u16* __restrict__ q, const u16* __restrict__ k,
                                               const u16* __restrict__ v, const u16* __restrict__ shk,
                                               const u16* __restrict__ cvec, const u16* __restrict__ wmod,
                                               u16* __restrict__ kvT, u16* __restrict__ xT,
                                               float* __restrict__ xstat, float* __restrict__ modf,
                                               float* __restrict__ zsum) {
  __shared__ __attribute__((aligned(16))) char smem[2 * 64 * 72 * 2];
  const int b = blockIdx.x, tid = threadIdx.x;
  if (b < 224) {
    u16* Tkv = (u16*)smem;
    u16* Tx = Tkv + 64 * 72;
    const int st = b / 7, dt = b % 7;
    const int s0 = st * 64, d0 = dt * 64;
    const int c = tid & 7, r = tid >> 3;
    u16 sh[32];
    u16 qq[2][8], vv[2][8], kk[2][4][8];
    {
      const u32x4* sp = (const u32x4*)(shk + (size_t)(d0 + 8 * c) * 4);
#pragma unroll
      for (int i = 0; i < 4; i++) *(u32x4*)&sh[8 * i] = sp[i];
    }
#pragma unroll
    for (int pass = 0; pass < 2; pass++) {
      const int s = s0 + r + 32 * pass;
      *(u32x4*)qq[pass] = *(const u32x4*)(q + (size_t)s * D + d0 + 8 * c);
      *(u32x4*)vv[pass] = *(const u32x4*)(v + (size_t)s * D + d0 + 8 * c);
#pragma unroll
      for (int j = 0; j < 4; j++) {
        const int sj = s - j >= 0 ? s - j : 0;
        u32x4 t = *(const u32x4*)(k + (size_t)sj * D + d0 + 8 * c);
        if (s - j < 0) t = (u32x4){0, 0, 0, 0};
        *(u32x4*)kk[pass][j] = t;
      }
    }
#pragma unroll
    for (int pass = 0; pass < 2; pass++) {
      const int row = r + 32 * pass;
#pragma unroll
      for (int e = 0; e < 8; e++) {
        float acc = 0.f;
#pragma unroll
        for (int j = 3; j >= 0; j--) acc = rb(acc + rb(bf2f(kk[pass][j][e]) * bf2f(sh[e * 4 + j])));
        Tkv[(8 * c + e) * 72 + row] = f2bf(acc * bf2f(vv[pass][e]));
        Tx[(8 * c + e) * 72 + row] = qq[pass][e];
      }
    }
    __syncthreads();
#pragma unroll
    for (int pass = 0; pass < 2; pass++) {
      const int dr = r + 32 * pass;
      *(u32x4*)(kvT + (size_t)(d0 + dr) * S + s0 + 8 * c) = *(const u32x4*)(Tkv + dr * 72 + 8 * c);
      *(u32x4*)(xT + (size_t)(d0 + dr) * S + s0 + 8 * c) = *(const u32x4*)(Tx + dr * 72 + 8 * c);
    }
  } else if (b < 736) {
    const int wave = tid >> 6, lane = tid & 63;
    const int s = (b - 224) * 4 + wave;
    float xv[8];
    float sum = 0.f;
    if (lane < 56) {
      u16 t[8];
      *(u32x4*)t = *(const u32x4*)(q + (size_t)s * D + lane * 8);
#pragma unroll
      for (int e = 0; e < 8; e++) { xv[e] = bf2f(t[e]); sum += xv[e]; }
    } else {
#pragma unroll
      for (int e = 0; e < 8; e++) xv[e] = 0.f;
    }
#pragma unroll
    for (int o = 32; o >= 1; o >>= 1) sum += __shfl_xor(sum, o);
    const float mean = sum * (1.f / D);
    float sq = 0.f;
    if (lane < 56) {
#pragma unroll
      for (int e = 0; e < 8; e++) { float dd = xv[e] - mean; sq += dd * dd; }
    }
#pragma unroll
    for (int o = 32; o >= 1; o >>= 1) sq += __shfl_xor(sq, o);
    if (lane == 0) {
      const float var = rb(sq * (1.f / D));
      xstat[2 * s] = rb(mean);
      xstat[2 * s + 1] = 1.f / rb(sqrtf(rb(var + 1e-5f)));
    }
  } else {
    float* red = (float*)smem;
    const int n0 = (b - 736) * 32;
    const int c4 = tid & 3, ks = tid >> 2;
    u32x4 t[7];
    float cv[7];
#pragma unroll
    for (int kk = 0; kk < 7; kk++) {
      const int kr = ks * 7 + kk;
      t[kk] = *(const u32x4*)(wmod + (size_t)kr * (6 * D) + n0 + 8 * c4);
      cv[kk] = bf2f(cvec[kr]);
    }
    float acc[8];
#pragma unroll
    for (int e = 0; e < 8; e++) acc[e] = 0.f;
#pragma unroll
    for (int kk = 0; kk < 7; kk++) {
      const float sc = rb(cv[kk] / (1.f + __expf(-cv[kk])));
      u16 tt[8];
      *(u32x4*)tt = t[kk];
#pragma unroll
      for (int e = 0; e < 8; e++) acc[e] += sc * bf2f(tt[e]);
    }
#pragma unroll
    for (int e = 0; e < 8; e++) red[ks * 32 + c4 * 8 + e] = acc[e];
    __syncthreads();
    if (tid < 32) {
      float sum = 0.f;
#pragma unroll
      for (int i = 0; i < 64; i++) sum += red[i * 32 + tid];
      modf[n0 + tid] = rb(sum);
    }
  }
}

#define RSK(i) ((i) + (((i) >> 7) << 3))
__global__ __launch_bounds__(256) void k2_conv(const u16* __restrict__ kvT, const u16* __restrict__ xT,
                                               const u16* __restrict__ ssmk, const u16* __restrict__ ssmD,
                                               const float* __restrict__ xstat, const float* __restrict__ modf,
                                               u16* __restrict__ x1T) {
  __shared__ __attribute__((aligned(16))) u16 KFs[2208];
  __shared__ __attribute__((aligned(16))) u16 Rs[4112 + 8 * 33];
  __shared__ __attribute__((aligned(16))) float ybuf[4][2112];
  const int d = blockIdx.x, tid = threadIdx.x;
  const int wave = tid >> 6, lane = tid & 63;
  u16 kvv[8], xv[8];
  f32x4 st[4];
  const u32x4 kf = *(const u32x4*)(ssmk + (size_t)d * S + 8 * tid);
  *(u32x4*)kvv = *(const u32x4*)(kvT + (size_t)d * S + 8 * tid);
  *(u32x4*)xv = *(const u32x4*)(xT + (size_t)d * S + 8 * tid);
#pragma unroll
  for (int i = 0; i < 4; i++) st[i] = *(const f32x4*)(xstat + 16 * tid + 4 * i);
  const float Dd = bf2f(ssmD[d]);
  const float sa = modf[d], opca = rb(1.f + modf[D + d]), ga = modf[2 * D + d];
  {
    u32x4 z = {0, 0, 0, 0};
    if (tid < 16) *(u32x4*)&KFs[8 * tid] = z;
    if (tid < 4) *(u32x4*)&KFs[2176 + 8 * tid] = z;
    *(u32x4*)&KFs[128 + 8 * tid] = kf;
    u16 tr[8];
#pragma unroll
    for (int e = 0; e < 8; e++) tr[7 - e] = kvv[e];
    *(u32x4*)&Rs[RSK(2040 - 8 * tid + 8)] = *(u32x4*)tr;
    if (tid == 0) *(u32x4*)&Rs[0] = z;
    *(u32x4*)&Rs[RSK(2056 + 8 * tid)] = z;
    if (tid == 0) *(u32x4*)&Rs[RSK(2056 + 8 * 256)] = z;
  }
  __syncthreads();
  const int m = lane & 15, h = lane >> 4;
  f32x4 acc[8];
#pragma unroll
  for (int p = 0; p < 8; p++) acc[p] = (f32x4){0.f, 0.f, 0.f, 0.f};
  const int tbeg = wave == 0 ? 0 : 17 + 16 * (wave - 1);
  const int tend = wave == 0 ? 17 : tbeg + 16;
#pragma unroll 2
  for (int t = tbeg; t < tend; t++) {
    const int q0 = -128 + 32 * t;
    const s16x8 a = *(const s16x8*)&KFs[8 * m + q0 + 8 * h + 128];
    const int basep = 2040 - 128 * m + q0 + 8 * h;
    const u32x4 w0 = *(const u32x4*)&Rs[RSK(basep + 8)];
    const u32x4 w1 = *(const u32x4*)&Rs[RSK(basep + 16)];
    unsigned W[8] = {w0[0], w0[1], w0[2], w0[3], w1[0], w1[1], w1[2], w1[3]};
#pragma unroll
    for (int p = 0; p < 8; p++) {
      const int sft = 7 - p;
      u32x4 fr;
      if ((sft & 1) == 0) {
#pragma unroll
        for (int i = 0; i < 4; i++) fr[i] = W[sft / 2 + i];
      } else {
#pragma unroll
        for (int i = 0; i < 4; i++) fr[i] = __builtin_amdgcn_alignbit(W[(sft - 1) / 2 + i + 1], W[(sft - 1) / 2 + i], 16);
      }
      acc[p] = __builtin_amdgcn_mfma_f32_16x16x32_bf16(a, *(s16x8*)&fr, acc[p], 0, 0, 0);
    }
  }
  {
    const int i = lane & 15;
#pragma unroll
    for (int reg = 0; reg < 4; reg++) {
      const int mm = 4 * h + reg;
      const int s = 128 * i + 8 * mm;
      f32x4 v0 = {acc[0][reg], acc[1][reg], acc[2][reg], acc[3][reg]};
      f32x4 v1 = {acc[4][reg], acc[5][reg], acc[6][reg], acc[7][reg]};
      *(f32x4*)&ybuf[wave][s + 4 * i] = v0;
      *(f32x4*)&ybuf[wave][s + 4 * i + 4] = v1;
    }
  }
  __syncthreads();
  {
    const int s0 = 8 * tid;
    const int idx = s0 + 4 * (s0 >> 7);
    float y[8];
#pragma unroll
    for (int e = 0; e < 8; e++) y[e] = 0.f;
#pragma unroll
    for (int w = 0; w < 4; w++) {
      f32x4 v0 = *(const f32x4*)&ybuf[w][idx];
      f32x4 v1 = *(const f32x4*)&ybuf[w][idx + 4];
#pragma unroll
      for (int e = 0; e < 4; e++) { y[e] += v0[e]; y[4 + e] += v1[e]; }
    }
    u16 o[8];
#pragma unroll
    for (int e = 0; e < 8; e++) {
      const float mean = st[e >> 1][(e & 1) * 2], sd = st[e >> 1][(e & 1) * 2 + 1];
      const float yy = rb(y[e]);
      const float y2 = rb(yy + rb(bf2f(kvv[e]) * Dd));
      const float x = bf2f(xv[e]);
      const float ln = rb(rb(x - mean) * sd);
      const float hh = rb(rb(ln * opca) + sa);
      const float a = rb(hh * y2);
      o[e] = f2bf(x + rb(ga * a));
    }
    *(u32x4*)(x1T + (size_t)d * S + s0) = *(u32x4*)o;
  }
}

template <int NI>
__device__ __forceinline__ void stage_kc(const u16* __restrict__ g, int ld, char* lds, int tid) {
#pragma unroll
  for (int i = 0; i < NI; i++) {
    const int b = i * 4096 + tid * 16;
    const int row = b >> 7, ch = (b >> 4) & 7;
    const int sch = ch ^ (row & 7);
    __builtin_amdgcn_global_load_lds((const unsigned*)(g + (size_t)row * ld + sch * 8), (unsigned*)(lds + b), 16, 0, 0);
  }
}
template <int ROWB, int NI>
__device__ __forceinline__ void stage_kn(const u16* __restrict__ g, int ld, char* lds, int tid) {
#pragma unroll
  for (int i = 0; i < NI; i++) {
    const int b = i * 4096 + tid * 16;
    const int row = b / ROWB, ch = (b % ROWB) >> 4;
    const int sch = ch ^ swzB<ROWB>(row);
    __builtin_amdgcn_global_load_lds((const unsigned*)(g + (size_t)row * ld + sch * 8), (unsigned*)(lds + b), 16, 0, 0);
  }
}

__device__ __forceinline__ float gelu_tanh(float x) {
  const float y = x * (2.302208198f + 0.1029432397f * x * x);
  const float e = __builtin_amdgcn_exp2f(-y);
  return x * __builtin_amdgcn_rcpf(1.f + e);
}

constexpr int K3_A = 448 * 128;
constexpr int K3_BST = 64 * 512;
__global__ __launch_bounds__(256) void k3_gemm1(const u16* __restrict__ x1T, const u16* __restrict__ win,
                                                const float* __restrict__ modf, u16* __restrict__ z,
                                                float* __restrict__ zsum) {
  __shared__ __attribute__((aligned(16))) char smem[K3_A + 3 * K3_BST];
  char* As = smem;
  char* Bs = smem + K3_A;
  const int tid = threadIdx.x, wave = tid >> 6, lane = tid & 63;
  const int m0 = (blockIdx.x & 31) * 64, n0 = (blockIdx.x >> 5) * 256;
  stage_kn<512, 8>(win + n0, F, Bs, tid);
  stage_kn<512, 8>(win + (size_t)64 * F + n0, F, Bs + K3_BST, tid);
  {
    const int c = tid & 7, r = tid >> 3;
    u32x4 xr[14];
#pragma unroll
    for (int p = 0; p < 14; p++) xr[p] = *(const u32x4*)(x1T + (size_t)(r + 32 * p) * S + m0 + 8 * c);
    float s1[8], s2[8];
#pragma unroll
    for (int e = 0; e < 8; e++) { s1[e] = 0.f; s2[e] = 0.f; }
#pragma unroll
    for (int p = 0; p < 14; p++) {
      u16 t[8];
      *(u32x4*)t = xr[p];
#pragma unroll
      for (int e = 0; e < 8; e++) { float x = bf2f(t[e]); s1[e] += x; s2[e] += x * x; }
    }
#pragma unroll
    for (int e = 0; e < 8; e++) {
#pragma unroll
      for (int o = 8; o <= 32; o <<= 1) { s1[e] += __shfl_xor(s1[e], o); s2[e] += __shfl_xor(s2[e], o); }
    }
    float* red = (float*)(Bs + 2 * K3_BST);
    if (lane < 8) {
#pragma unroll
      for (int e = 0; e < 8; e++) { red[wave * 128 + (c * 8 + e) * 2] = s1[e]; red[wave * 128 + (c * 8 + e) * 2 + 1] = s2[e]; }
    }
    __syncthreads();
    float mu[8], rs[8];
#pragma unroll
    for (int e = 0; e < 8; e++) {
      const int col = c * 8 + e;
      const float a1 = red[col * 2] + red[128 + col * 2] + red[256 + col * 2] + red[384 + col * 2];
      const float a2 = red[col * 2 + 1] + red[128 + col * 2 + 1] + red[256 + col * 2 + 1] + red[384 + col * 2 + 1];
      const float mean = a1 * (1.f / D);
      const float var = rb(fmaxf(a2 * (1.f / D) - mean * mean, 0.f));
      mu[e] = rb(mean);
      rs[e] = 1.f / rb(sqrtf(rb(var + 1e-5f)));
    }
#pragma unroll
    for (int p = 0; p < 14; p++) {
      const int kr = r + 32 * p;
      const float opc = rb(1.f + modf[4 * D + kr]), sb = modf[3 * D + kr];
      u32x4 o;
#pragma unroll
      for (int e = 0; e < 4; e++) {
        const float h0 = (lo16(xr[p][e]) - mu[2 * e]) * rs[2 * e] * opc + sb;
        const float h1 = (hi16(xr[p][e]) - mu[2 * e + 1]) * rs[2 * e + 1] * opc + sb;
        o[e] = pk2(h0, h1);
      }
      *(u32x4*)(As + kr * 128 + ((c ^ swzB<128>(kr)) << 4)) = o;
    }
  }
  const int wr = wave >> 1, wc = wave & 1;
  f32x4 acc[2][8];
#pragma unroll
  for (int i = 0; i < 2; i++)
#pragma unroll
    for (int j = 0; j < 8; j++) acc[i][j] = (f32x4){0.f, 0.f, 0.f, 0.f};
  for (int kt = 0; kt < 7; kt++) {
    if (kt < 6) { WAIT_V(8); } else { WAIT_V(0); }
    asm volatile("s_waitcnt lgkmcnt(0)" ::: "memory");
    BAR();
    if (kt + 2 < 7) stage_kn<512, 8>(win + (size_t)(kt + 2) * 64 * F + n0, F, Bs + ((kt + 2) % 3) * K3_BST, tid);
    const char* Bt = Bs + (kt % 3) * K3_BST;
#pragma unroll
    for (int ks = 0; ks < 2; ks++) {
      s16x8 af[2], bfr[8];
      TrF ta[2], tb[8];
#pragma unroll
      for (int i = 0; i < 2; i++) tr_issue<128>(ta[i], As, kt * 64 + ks * 32, wr * 32 + i * 16, lane);
#pragma unroll
      for (int j = 0; j < 8; j++) tr_issue<512>(tb[j], Bt, ks * 32, wc * 128 + j * 16, lane);
      TRW2(ta[0], ta[1]); TRW4(tb[0], tb[1], tb[2], tb[3]); TRW4(tb[4], tb[5], tb[6], tb[7]);
#pragma unroll
      for (int i = 0; i < 2; i++) af[i] = tr_get(ta[i]);
#pragma unroll
      for (int j = 0; j < 8; j++) bfr[j] = tr_get(tb[j]);
#pragma unroll
      for (int i = 0; i < 2; i++)
#pragma unroll
        for (int j = 0; j < 8; j++) acc[i][j] = __builtin_amdgcn_mfma_f32_16x16x32_bf16(af[i], bfr[j], acc[i][j], 0, 0, 0);
    }
  }
  __syncthreads();
  u16* Ct = (u16*)smem;
  const int fr = lane & 15, fq = lane >> 4;
#pragma unroll
  for (int i = 0; i < 2; i++) {
#pragma unroll
    for (int reg = 0; reg < 4; reg++) {
      const int row = wr * 32 + i * 16 + fq * 4 + reg;
      float s1 = 0.f, s2 = 0.f;
#pragma unroll
      for (int j = 0; j < 8; j++) {
        const float g = rb(gelu_tanh(rb(acc[i][j][reg])));
        Ct[row * 264 + wc * 128 + j * 16 + fr] = f2bf(g);
        s1 += g; s2 += g * g;
      }
      if (n0 + 256 > FH) {
        const int cbase = n0 + wc * 128;
        if (cbase >= FH) {
#pragma unroll
          for (int o = 1; o < 16; o <<= 1) { s1 += __shfl_xor(s1, o); s2 += __shfl_xor(s2, o); }
          if (fr == 0) { const int slot = (cbase - FH) >> 7; zsum[(m0 + row) * 14 + 2 * slot] = s1; zsum[(m0 + row) * 14 + 2 * slot + 1] = s2; }
        }
      }
    }
  }
  __syncthreads();
#pragma unroll
  for (int p = 0; p < 8; p++) {
    const int idx = p * 256 + tid;
    const int row = idx >> 5, ch = idx & 31;
    *(u32x4*)(z + (size_t)(m0 + row) * F + n0 + ch * 8) = *(const u32x4*)(Ct + row * 264 + ch * 8);
  }
}

__global__ __launch_bounds__(256) void k3b_norm(const u16* __restrict__ z, const float* __restrict__ zsum,
                                                u16* __restrict__ z2n) {
  const int idx = blockIdx.x * 256 + threadIdx.x;
  const int row = idx / 112, ch = idx % 112;
  u16 t[8];
  *(u32x4*)t = *(const u32x4*)(z + (size_t)row * F + FH + ch * 8);
  float s1 = 0.f, s2 = 0.f;
#pragma unroll
  for (int i = 0; i < 7; i++) { s1 += zsum[row * 14 + 2 * i]; s2 += zsum[row * 14 + 2 * i + 1]; }
  const float mean = s1 * (1.f / FH);
  const float var = rb(fmaxf(s2 * (1.f / FH) - mean * mean, 0.f));
  const float mu = rb(mean), sd = 1.f / rb(sqrtf(rb(var + 1e-5f)));
#pragma unroll
  for (int e = 0; e < 8; e++) t[e] = f2bf(rb(bf2f(t[e]) - mu) * sd);
  *(u32x4*)(z2n + (size_t)row * FH + ch * 8) = *(u32x4*)t;
}

constexpr int K4_ST = 128 * 128 + 64 * 128;
template <int NI>
__device__ __forceinline__ void stage_kc512(const u16* __restrict__ g, int ld, char* lds, int tid) {
#pragma unroll
  for (int i = 0; i < NI; i++) {
    const int b = i * 8192 + tid * 16;
    const int row = b >> 7, ch = (b >> 4) & 7;
    const int sch = ch ^ (row & 7);
    __builtin_amdgcn_global_load_lds((const unsigned*)(g + (size_t)row * ld + sch * 8), (unsigned*)(lds + b), 16, 0, 0);
  }
}
template <int ROWB, int NI>
__device__ __forceinline__ void stage_kn512(const u16* __restrict__ g, int ld, char* lds, int tid) {
#pragma unroll
  for (int i = 0; i < NI; i++) {
    const int b = i * 8192 + tid * 16;
    const int row = b / ROWB, ch = (b % ROWB) >> 4;
    const int sch = ch ^ swzB<ROWB>(row);
    __builtin_amdgcn_global_load_lds((const unsigned*)(g + (size_t)row * ld + sch * 8), (unsigned*)(lds + b), 16, 0, 0);
  }
}
__global__ __launch_bounds__(512) void k4_gemm2(const u16* __restrict__ wsp, const u16* __restrict__ z2n,
                                                const u16* __restrict__ z, const u16* __restrict__ bsp,
                                                u16* __restrict__ u) {
  __shared__ __attribute__((aligned(16))) char smem[4 * K4_ST];
  const int tid = threadIdx.x, wave = tid >> 6, lane = tid & 63;
  const int b = blockIdx.x;
  const int xcd = b & 7, idx = b >> 3;
  const int mt = (xcd >> 1) * 4 + (idx & 3), nt = (xcd & 1) * 7 + (idx >> 2);
  const int m0 = mt * 128, n0 = nt * 64;
  const int koff = 0;
  const int wr = wave >> 1, wc = wave & 1;
  f32x4 acc[2][2];
#pragma unroll
  for (int i = 0; i < 2; i++)
#pragma unroll
    for (int j = 0; j < 2; j++) acc[i][j] = (f32x4){0.f, 0.f, 0.f, 0.f};
  auto issue = [&](int kt) {
    char* st = smem + (kt & 3) * K4_ST;
    const int ke = (kt + koff) & 31;
    stage_kc512<2>(wsp + (size_t)m0 * S + ke * 64, S, st, tid);
    stage_kn512<128, 1>(z2n + (size_t)(ke * 64) * FH + n0, FH, st + 128 * 128, tid);
  };
  issue(0); issue(1); issue(2);
  for (int kt = 0; kt < 32; kt++) {
    if (kt < 30) { WAIT_V(6); } else if (kt == 30) { WAIT_V(3); } else { WAIT_V(0); }
    asm volatile("s_waitcnt lgkmcnt(0)" ::: "memory");
    BAR();
    if (kt + 3 < 32) issue(kt + 3);
    const char* At = smem + (kt & 3) * K4_ST;
    const char* Bt = At + 128 * 128;
#pragma unroll
    for (int ks = 0; ks < 2; ks++) {
      s16x8 af[2], bfr[2];
      TrF tb[2];
#pragma unroll
      for (int i = 0; i < 2; i++) {
        const int row = wr * 32 + i * 16 + (lane & 15);
        af[i] = *(const s16x8*)(At + row * 128 + (((ks * 4 + (lane >> 4)) ^ (row & 7)) << 4));
      }
#pragma unroll
      for (int j = 0; j < 2; j++) tr_issue<128>(tb[j], Bt, ks * 32, wc * 32 + j * 16, lane);
      TRW2(tb[0], tb[1]);
      bfr[0] = tr_get(tb[0]); bfr[1] = tr_get(tb[1]);
#pragma unroll
      for (int i = 0; i < 2; i++)
#pragma unroll
        for (int j = 0; j < 2; j++) acc[i][j] = __builtin_amdgcn_mfma_f32_16x16x32_bf16(af[i], bfr[j], acc[i][j], 0, 0, 0);
    }
  }
  __syncthreads();
  float* Ct = (float*)smem;
  const int fr = lane & 15, fq = lane >> 4;
#pragma unroll
  for (int i = 0; i < 2; i++)
#pragma unroll
    for (int reg = 0; reg < 4; reg++) {
      const int row = wr * 32 + i * 16 + fq * 4 + reg;
#pragma unroll
      for (int j = 0; j < 2; j++) Ct[row * 68 + wc * 32 + j * 16 + fr] = acc[i][j][reg];
    }
  __syncthreads();
#pragma unroll
  for (int p = 0; p < 2; p++) {
    const int id = p * 512 + tid;
    const int row = id >> 3, ch = id & 7;
    const float bb = bf2f(bsp[m0 + row]);
    u16 t[8];
    *(u32x4*)t = *(const u32x4*)(z + (size_t)(m0 + row) * F + n0 + ch * 8);
#pragma unroll
    for (int e = 0; e < 8; e++) {
      const float g = rb(rb(Ct[row * 68 + ch * 8 + e]) + bb);
      t[e] = f2bf(bf2f(t[e]) * g);
    }
    *(u32x4*)(u + (size_t)(m0 + row) * FH + n0 + ch * 8) = *(u32x4*)t;
  }
}

constexpr int K5_ST = 64 * 128 + 64 * 128;
__global__ __launch_bounds__(256) void k5_gemm3(const u16* __restrict__ u, const u16* __restrict__ wout,
                                                const u16* __restrict__ x1T, const float* __restrict__ modf,
                                                u16* __restrict__ out) {
  __shared__ __attribute__((aligned(16))) char smem[4 * K5_ST];
  const int tid = threadIdx.x, wave = tid >> 6, lane = tid & 63;
  const int b = blockIdx.x;
  const int xcd = b & 7, idx = b >> 3;
  const int mt = xcd * 4 + (idx & 3), nt = idx >> 2;
  const int m0 = mt * 64, n0 = nt * 64;
  const int wr = wave >> 1, wc = wave & 1;
  f32x4 acc[2][2];
#pragma unroll
  for (int i = 0; i < 2; i++)
#pragma unroll
    for (int j = 0; j < 2; j++) acc[i][j] = (f32x4){0.f, 0.f, 0.f, 0.f};
  auto issue = [&](int kt) {
    char* st = smem + (kt & 3) * K5_ST;
    stage_kc<2>(u + (size_t)m0 * FH + kt * 64, FH, st, tid);
    stage_kn<128, 2>(wout + (size_t)(kt * 64) * D + n0, D, st + 64 * 128, tid);
  };
  issue(0); issue(1); issue(2);
  for (int kt = 0; kt < 14; kt++) {
    if (kt < 12) { WAIT_V(8); } else if (kt == 12) { WAIT_V(4); } else { WAIT_V(0); }
    asm volatile("s_waitcnt lgkmcnt(0)" ::: "memory");
    BAR();
    if (kt + 3 < 14) issue(kt + 3);
    const char* At = smem + (kt & 3) * K5_ST;
    const char* Bt = At + 64 * 128;
#pragma unroll
    for (int ks = 0; ks < 2; ks++) {
      s16x8 af[2], bfr[2];
      TrF tb[2];
#pragma unroll
      for (int i = 0; i < 2; i++) {
        const int row = wr * 32 + i * 16 + (lane & 15);
        af[i] = *(const s16x8*)(At + row * 128 + (((ks * 4 + (lane >> 4)) ^ (row & 7)) << 4));
      }
#pragma unroll
      for (int j = 0; j < 2; j++) tr_issue<128>(tb[j], Bt, ks * 32, wc * 32 + j * 16, lane);
      TRW2(tb[0], tb[1]);
      bfr[0] = tr_get(tb[0]); bfr[1] = tr_get(tb[1]);
#pragma unroll
      for (int i = 0; i < 2; i++)
#pragma unroll
        for (int j = 0; j < 2; j++) acc[i][j] = __builtin_amdgcn_mfma_f32_16x16x32_bf16(af[i], bfr[j], acc[i][j], 0, 0, 0);
    }
  }
  __syncthreads();
  u16* Ct = (u16*)smem;
  const int fr = lane & 15, fq = lane >> 4;
#pragma unroll
  for (int i = 0; i < 2; i++)
#pragma unroll
    for (int j = 0; j < 2; j++) {
      const int col = wc * 32 + j * 16 + fr;
      const int row0 = wr * 32 + i * 16 + fq * 4;
      const float gb = modf[5 * D + n0 + col];
      u16 xv[4];
      *(uint2*)xv = *(const uint2*)(x1T + (size_t)(n0 + col) * S + m0 + row0);
#pragma unroll
      for (int reg = 0; reg < 4; reg++) Ct[(row0 + reg) * 72 + col] = f2bf(bf2f(xv[reg]) + rb(gb * rb(acc[i][j][reg])));
    }
  __syncthreads();
#pragma unroll
  for (int p = 0; p < 2; p++) {
    const int id = p * 256 + tid;
    const int row = id >> 3, ch = id & 7;
    *(u32x4*)(out + (size_t)(m0 + row) * D + n0 + ch * 8) = *(const u32x4*)(Ct + row * 72 + ch * 8);
  }
}

extern "C" void kernel_launch(void* const* d_in, const int* in_sizes, int n_in, void* d_out, int out_size,
                              void* d_ws, size_t ws_size, hipStream_t stream) {
  const u16* q = (const u16*)d_in[0];
  const u16* k = (const u16*)d_in[1];
  const u16* v = (const u16*)d_in[2];
  const u16* shk = (const u16*)d_in[3];
  const u16* ssmk = (const u16*)d_in[4];
  const u16* ssmD = (const u16*)d_in[5];
  const u16* win = (const u16*)d_in[7];
  const u16* wsp = (const u16*)d_in[8];
  const u16* bsp = (const u16*)d_in[9];
  const u16* wout = (const u16*)d_in[10];
  const u16* cvec = (const u16*)d_in[11];
  const u16* wmod = (const u16*)d_in[12];
  u16* out = (u16*)d_out;
  char* ws = (char*)d_ws;
  size_t off = 0;
  auto take = [&](size_t bytes) { char* p = ws + off; off += (bytes + 255) & ~(size_t)255; return p; };
  u16* kvT = (u16*)take((size_t)D * S * 2);
  u16* xT = (u16*)take((size_t)D * S * 2);
  u16* x1T = (u16*)take((size_t)D * S * 2);
  float* xstat = (float*)take((size_t)S * 2 * 4);
  float* modf = (float*)take((size_t)6 * D * 4);
  float* zsum = (float*)take((size_t)S * 14 * 4);
  u16* z = (u16*)take((size_t)S * F * 2);
  u16* z2n = (u16*)take((size_t)S * FH * 2);
  u16* u = (u16*)take((size_t)S * FH * 2);

  k1_prep<<<820, 256, 0, stream>>>(q, k, v, shk, cvec, wmod, kvT, xT, xstat, modf, zsum);
  k2_conv<<<D, 256, 0, stream>>>(kvT, xT, ssmk, ssmD, xstat, modf, x1T);
  k3_gemm1<<<32 * 7, 256, 0, stream>>>(x1T, win, modf, z, zsum);
  k3b_norm<<<896, 256, 0, stream>>>(z, zsum, z2n);
  k4_gemm2<<<16 * 14, 512, 0, stream>>>(wsp, z2n, z, bsp, u);
  k5_gemm3<<<32 * 7, 256, 0, stream>>>(u, wout, x1T, modf, out);
}
```
